```python
import torch

def _softmax(x, axis=-1):
    return torch.softmax(x, dim=axis)
def _gelu(x):
    return torch.nn.functional.gelu(x, approximate="tanh")
def _silu(x):
    return torch.nn.functional.silu(x)
def _sigmoid(x):
    return torch.sigmoid(x)
def _layernorm(x, axis=-1, eps=1e-5):
    m = x.mean(dim=axis, keepdim=True)
    v = x.var(dim=axis, keepdim=True, unbiased=False)
    return (x - m) / torch.sqrt(v + eps)
def _scatter_add(tgt, idx, src, axis=0):
    return tgt.clone().index_add_(axis, idx, src)
def _segment_sum(src, seg, n):
    out = torch.zeros((n,) + src.shape[1:], dtype=src.dtype)
    return out.index_add_(0, seg, src)
def _pad(x, pw):
    flat = tuple(v for pair in reversed(pw) for v in pair)
    return torch.nn.functional.pad(x, flat)

kernel_name = 'concat_heads__spectral_gfnet__spectral_gfnet__prompt'

def setup_inputs():

    _w = (lambda a: (lambda t: t.to(torch.bfloat16) if t.is_floating_point() else t)(torch.as_tensor(a)))
    def _A_setup():
        B, S, D = 1, 2048, 1024
        Df = D // 2 + 1
        return {
            "x": _w((lambda *s: torch.randn(*s, dtype=torch.bfloat16) * 0.05)(B, S, D)),
            "w_r": _w((lambda *s: torch.randn(*s, dtype=torch.bfloat16) * 0.05)(S, Df) * 0.02),
            "w_i": _w((lambda *s: torch.randn(*s, dtype=torch.bfloat16) * 0.05)(S, Df) * 0.02),
        }
    _A_in = _A_setup()

    def _B_setup():
        B, S, D = 1, 2048, 1024
        Df = D // 2 + 1
        return {
            "x": _w((lambda *s: torch.randn(*s, dtype=torch.bfloat16) * 0.05)(B, S, D)),
            "w_r": _w((lambda *s: torch.randn(*s, dtype=torch.bfloat16) * 0.05)(S, Df) * 0.02),
            "w_i": _w((lambda *s: torch.randn(*s, dtype=torch.bfloat16) * 0.05)(S, Df) * 0.02),
        }
    _B_in = _B_setup()

    return {
        "A_x": _A_in["x"],
        "A_w_r": _A_in["w_r"],
        "A_w_i": _A_in["w_i"],
        "B_x": _B_in["x"],
        "B_w_r": _B_in["w_r"],
        "B_w_i": _B_in["w_i"],
    }

def reference(A_x, A_w_r, A_w_i, B_x, B_w_r, B_w_i):
    def _A_ref(x, w_r, w_i):
        B, S, D = x.shape
        X = (lambda x, axes=(-2,-1): torch.fft.rfft2(x.float(), dim=axes))(x, axes=(-2, -1))
        W = w_r + 1j * w_i
        X = X * W
        y = (lambda x, s, axes=(-2,-1): torch.fft.irfft2(x, s=s, dim=axes).to(torch.bfloat16))(X, (S, D), axes=(-2, -1))
        return y

    def _B_ref(x, w_r, w_i):
        B, S, D = x.shape
        X = (lambda x, axes=(-2,-1): torch.fft.rfft2(x.float(), dim=axes))(x, axes=(-2, -1))
        W = w_r + 1j * w_i
        X = X * W
        y = (lambda x, s, axes=(-2,-1): torch.fft.irfft2(x, s=s, dim=axes).to(torch.bfloat16))(X, (S, D), axes=(-2, -1))
        return y

    a = _A_ref(A_x, A_w_r, A_w_i)
    b = _B_ref(B_x, B_w_r, B_w_i)
    return torch.concatenate([a, b], axis=-1)
```

```cpp
#include <hip/hip_runtime.h>
#include <stdint.h>

#define FUSED 1

typedef __bf16 bf16x8 __attribute__((ext_vector_type(8)));
typedef __bf16 bf16x2 __attribute__((ext_vector_type(2)));
typedef float f32x4 __attribute__((ext_vector_type(4)));
typedef float f32x2 __attribute__((ext_vector_type(2)));
typedef unsigned u32x4 __attribute__((ext_vector_type(4)));
typedef unsigned u32x2 __attribute__((ext_vector_type(2)));
typedef unsigned long long u64;
#define DI __device__ __forceinline__
#define GAS __attribute__((address_space(1)))
#define LAS __attribute__((address_space(3)))

constexpr int DF = 513;
constexpr unsigned BUF_BYTES = 2u * 128u * 128u * 256u;
constexpr unsigned FLAG_BYTES = 2u * 128u * 128u * 8u;
constexpr unsigned WS_BUF1 = 0, WS_BUF2 = BUF_BYTES, WS_FLAG1 = 2 * BUF_BYTES, WS_FLAG2 = WS_FLAG1 + FLAG_BYTES,
                   WS_EPOCH = WS_FLAG2 + FLAG_BYTES;

DI unsigned pk(float lo, float hi) { f32x2 v = {lo, hi}; return __builtin_bit_cast(unsigned, __builtin_convertvector(v, bf16x2)); }
DI f32x4 mfma(u32x4 a, u32x4 b, f32x4 c) {
    return __builtin_amdgcn_mfma_f32_16x16x32_bf16(__builtin_bit_cast(bf16x8, a), __builtin_bit_cast(bf16x8, b), c, 0, 0, 0);
}
DI float cos2048(int n) { return __builtin_amdgcn_cosf((float)(n & 2047) * (1.0f / 2048.0f)); }
DI float sin2048(int n) { return __builtin_amdgcn_sinf((float)(n & 2047) * (1.0f / 2048.0f)); }

DI float coef1(int sg, int ri, int a) { return cos2048(sg ? 64 * a * sg + 512 * ri : 1024 * a * ri); }
DI float coef8(int J, int k, int ro, int n, int ri, int extra) {
    int ph = n * (J + 256 * k) + 512 * (ro - ri);
    bool ok = true;
    if (J == 0) { ok = ri == (k & 1); ph = k ? 128 * n * k + 512 * ro : 1024 * n * ro; }
    float v = cos2048(ph + extra);
    return ok ? v : 0.0f;
}

struct Args { const uint16_t* x[2]; const uint16_t* wr[2]; const uint16_t* wi[2]; uint16_t* out; char* ws; };

DI __amdgpu_buffer_rsrc_t rsrc(char* p, unsigned bytes) { return __builtin_amdgcn_make_buffer_rsrc(p, 0, bytes, 0x00020000); }
constexpr int AUX_X = FUSED ? 16 : 0;

DI void xstore(char* buf, int h, int q, int wv, int l, int g, const f32x4 (&re)[4], const f32x4 (&im)[4]) {
    auto rs = rsrc(buf, BUF_BYTES);
    unsigned off = ((h * 128 + 16 * wv + 4 * g) * 128 + q) * 256 + l * 16;
#pragma unroll
    for (int r = 0; r < 4; ++r) {
        u32x4 v = {pk(re[0][r], im[0][r]), pk(re[1][r], im[1][r]), pk(re[2][r], im[2][r]), pk(re[3][r], im[3][r])};
        __builtin_amdgcn_raw_buffer_store_b128(v, rs, off + r * 32768, 0, AUX_X);
    }
}
DI void xload(char* buf, int h, int q, int wv, int l, int g, u32x4 (&f)[4]) {
    auto rs = rsrc(buf, BUF_BYTES);
    unsigned off = ((h * 128 + q) * 128 + 16 * wv + 4 * g) * 256 + l * 16;
    u32x4 p[4];
#pragma unroll
    for (int k = 0; k < 4; ++k) p[k] = __builtin_amdgcn_raw_buffer_load_b128(rs, off + k * 256, 0, AUX_X);
#pragma unroll
    for (int i = 0; i < 4; ++i) f[i] = u32x4{p[0][i], p[1][i], p[2][i], p[3][i]};
}
DI void lds_read_frags(LAS char* s, int wv, int lane, u32x4 (&f)[4]) {
    LAS char* b = s + wv * 4096 + lane * 8;
#pragma unroll
    for (int i = 0; i < 4; ++i) {
        u32x2 a = *(LAS u32x2*)(b + i * 1024), c = *(LAS u32x2*)(b + i * 1024 + 512);
        f[i] = u32x4{a[0], a[1], c[0], c[1]};
    }
}

struct P1In { u32x2 xr[8]; };
DI void p1_load(const Args& A, int h, int q, int wv, int l, int g, P1In& in) {
    int c = (q >> 1) & 7, dt = 8 * (q & 1) + (q >> 4);
    const GAS char* xp = (const GAS char*)A.x[h] + ((size_t)(512 * g + 8 * wv + c) * 1024 + 64 * dt + 4 * l) * 2;
#pragma unroll
    for (int j = 0; j < 8; ++j) in.xr[j] = *(const GAS u32x2*)(xp + (size_t)j * 64 * 2048);
}
DI void p1_run(const Args& A, int h, int q, int wv, int lane, const P1In& in, LAS char* s) {
    const int l = lane & 15, g = lane >> 4;
    u32x4 A1[2], A2[2];
#pragma unroll
    for (int t = 0; t < 2; ++t)
#pragma unroll
        for (int k = 0; k < 4; ++k) {
            A1[t][k] = pk(coef1(8 * t + (l >> 1), l & 1, 8 * g + 2 * k), coef1(8 * t + (l >> 1), l & 1, 8 * g + 2 * k + 1));
            const int z = k & 1, b = 2 * g + (k >> 1), J = 8 * (2 * wv + z);
            unsigned v = pk(coef8(J, l & 7, t, b, 0, 0), coef8(J, l & 7, t, b, 1, 0));
            A2[t][k] = (l >> 3) == z ? v : 0u;
        }
    const f32x4 Z = {0.f, 0.f, 0.f, 0.f};
    u32x2 gr[2][4];
#pragma unroll
    for (int i = 0; i < 4; ++i) {
        u32x4 f;
#pragma unroll
        for (int k = 0; k < 4; ++k)
            f[k] = __builtin_amdgcn_perm(in.xr[2 * k + 1][i >> 1], in.xr[2 * k][i >> 1], (i & 1) ? 0x07060302u : 0x05040100u);
#pragma unroll
        for (int t = 0; t < 2; ++t) { f32x4 d = mfma(A1[t], f, Z); gr[t][i] = u32x2{pk(d[0], d[1]), pk(d[2], d[3])}; }
    }
    {
        LAS char* b = s + g * 4096 + (l >> 3) * 2048 + (wv & 1) * 512 + (wv >> 1) * 128 + (l & 7) * 16;
#pragma unroll
        for (int t = 0; t < 2; ++t)
#pragma unroll
            for (int i0 = 0; i0 < 2; ++i0)
                *(LAS u32x4*)(b + t * 16384 + i0 * 1024) = u32x4{gr[t][i0][0], gr[t][i0][1], gr[t][i0 + 2][0], gr[t][i0 + 2][1]};
    }
    __syncthreads();
    u32x4 f[4];
    lds_read_frags(s, wv, lane, f);
    f32x4 re[4], im[4];
#pragma unroll
    for (int i = 0; i < 4; ++i) { re[i] = mfma(A2[0], f[i], Z); im[i] = mfma(A2[1], f[i], Z); }
    xstore(A.ws + WS_BUF1, h, q, wv, l, g, re, im);
}

struct WIn { unsigned r[2][4], i[2][4]; uint16_t er[2], ei[2]; };
typedef unsigned __attribute__((aligned(2))) u32_a2;
DI int tileJ(int q) { int sg = 2 * (q >> 4) + ((q >> 3) & 1), kb = q & 7; return sg ? sg + 32 * kb : 16 * kb; }
DI void w_load(const Args& A, int h, int q, int wv, int l, int g, WIn& w) {
    const int J = tileJ(q);
    int k1 = J ? J + 256 * wv : 128 * wv;
    int km = k1 ? 2048 - k1 : 1024;
    const GAS uint16_t* pr = (const GAS uint16_t*)A.wr[h];
    const GAS uint16_t* pi = (const GAS uint16_t*)A.wi[h];
    const int o0 = k1 * DF + 2 * l + 128 * g, o1 = km * DF + 511 - 2 * l - 128 * g;
#pragma unroll
    for (int r = 0; r < 4; ++r) {
        w.r[0][r] = *(const GAS u32_a2*)(pr + o0 + 32 * r); w.i[0][r] = *(const GAS u32_a2*)(pi + o0 + 32 * r);
        w.r[1][r] = *(const GAS u32_a2*)(pr + o1 - 32 * r); w.i[1][r] = *(const GAS u32_a2*)(pi + o1 - 32 * r);
    }
    w.er[0] = pr[km * DF]; w.ei[0] = pi[km * DF]; w.er[1] = pr[k1 * DF + 512]; w.ei[1] = pi[k1 * DF + 512];
}
DI float lo16(unsigned v) { return __builtin_bit_cast(float, v << 16); }
DI float hi16(unsigned v) { return __builtin_bit_cast(float, v & 0xffff0000u); }

struct P2Const {
    u32x4 Aa[2], Ai[2], E0[2], R1[2], C1[2];
    float tc[2][2][4], ts[2][2][4];
    float wr[2][2][4], wi[2][2][4];
    float sp[2][2];
};
DI void p2_const(int q, int wv, int lane, const WIn& w, P2Const& C) {
    const int l = lane & 15, g = lane >> 4, J = tileJ(q);
#pragma unroll
    for (int k = 0; k < 4; ++k) {
        const int m = 4 * g + k;
#pragma unroll
        for (int ro = 0; ro < 2; ++ro) {
            const int ex = 1024 * (m & l & 1);
            C.Aa[ro][k] = pk(coef8(J, l >> 1, ro, m >> 1, 0, ex), coef8(J, l >> 1, ro, m >> 1, 1, ex));
            C.Ai[ro][k] = pk(coef8(J, m >> 1, 0, l >> 1, ro, ex), coef8(J, m >> 1, 1, l >> 1, ro, ex));
        }
        float c = cos2048(128 * l * m), s = sin2048(128 * l * m);
        C.E0[0][k] = pk(c, s); C.E0[1][k] = pk(-s, c);
        c = cos2048(64 * l * (1 + 2 * m)); s = sin2048(64 * l * (1 + 2 * m));
        C.R1[0][k] = pk(c, s); C.R1[1][k] = pk(-s, c);
        c = cos2048(64 * m * (1 + 2 * l)); s = sin2048(64 * m * (1 + 2 * l));
        C.C1[0][k] = pk(c, s); C.C1[1][k] = pk(-s, c);
#pragma unroll
        for (int ta = 0; ta < 2; ++ta)
#pragma unroll
            for (int ke = 0; ke < 2; ++ke) {
                const int n = 2 * (ta + 2 * m) * (ke + 2 * l);
                C.tc[ta][ke][k] = cos2048(n); C.ts[ta][ke][k] = sin2048(n);
            }
    }
    const float sc = 2.0f / (2048.0f * 1024.0f);
#pragma unroll
    for (int r = 0; r < 4; ++r) {
        C.wr[0][0][r] = lo16(w.r[0][r]) * sc; C.wr[0][1][r] = hi16(w.r[0][r]) * sc;
        C.wi[0][0][r] = lo16(w.i[0][r]) * sc; C.wi[0][1][r] = hi16(w.i[0][r]) * sc;
        C.wr[1][0][r] = hi16(w.r[1][r]) * sc; C.wr[1][1][r] = lo16(w.r[1][r]) * sc;
        C.wi[1][0][r] = -hi16(w.i[1][r]) * sc; C.wi[1][1][r] = -lo16(w.i[1][r]) * sc;
    }
    const float e0r = lo16(w.er[0]) * sc, e0i = -lo16(w.ei[0]) * sc, e1r = lo16(w.er[1]) * sc, e1i = lo16(w.ei[1]) * sc;
    C.sp[0][0] = 0.5f * C.wr[0][0][0]; C.sp[0][1] = 0.5f * e0r;
    C.sp[1][0] = 0.5f * e1r; C.sp[1][1] = 0.5f * C.wr[1][0][0];
    if (lane == 0) {
        C.wr[0][0][0] = 0.5f * (C.wr[0][0][0] + e0r); C.wi[0][0][0] = 0.5f * (C.wi[0][0][0] + e0i);
        C.wr[1][0][0] = 0.5f * (C.wr[1][0][0] + e1r); C.wi[1][0][0] = 0.5f * (C.wi[1][0][0] + e1i);
    }
}
DI u32x4 pack4(const f32x4& a, const f32x4& b) { return u32x4{pk(a[0], b[0]), pk(a[1], b[1]), pk(a[2], b[2]), pk(a[3], b[3])}; }
DI u32x4 neg4(u32x4 a) { return a ^ 0x80008000u; }
DI void twid(f32x4& re, f32x4& im, const float (&c)[4], const float (&s)[4]) {
#pragma unroll
    for (int r = 0; r < 4; ++r) { float a = re[r], b = im[r]; re[r] = a * c[r] + b * s[r]; im[r] = b * c[r] - a * s[r]; }
}
DI void mirror(LAS char* s, int l, int g, f32x4 (&re)[2][2], f32x4 (&im)[2][2], float s0, float scale) {
    const int k0 = 2 * l + 128 * g;
#pragma unroll
    for (int o = 0; o < 2; ++o)
#pragma unroll
        for (int ke = 0; ke < 2; ++ke)
#pragma unroll
            for (int r = 0; r < 4; ++r) *(LAS f32x2*)(s + (k0 + ke + 32 * r + 512 * o) * 8) = f32x2{re[o][ke][r], im[o][ke][r]};
    __builtin_amdgcn_fence(__ATOMIC_ACQ_REL, "wavefront");
    __builtin_amdgcn_wave_barrier();
#pragma unroll
    for (int o = 0; o < 2; ++o)
#pragma unroll
        for (int ke = 0; ke < 2; ++ke)
#pragma unroll
            for (int r = 0; r < 4; ++r) {
                f32x2 m = *(LAS f32x2*)(s + ((1024 - (k0 + ke + 32 * r + 512 * o)) & 1023) * 8);
                const float sg = o ? -s0 : s0;
                re[o][ke][r] = scale * (re[o][ke][r] + sg * m[0]); im[o][ke][r] = scale * (im[o][ke][r] - sg * m[1]);
            }
    __builtin_amdgcn_fence(__ATOMIC_ACQ_REL, "wavefront");
    __builtin_amdgcn_wave_barrier();
}
DI void p2_run(const Args& A, int h, int q, int wv, int lane, const u32x4 (&fin)[4], const P2Const& C, LAS char* s0, LAS char* s1, LAS char* sx) {
    const int l = lane & 15, g = lane >> 4;
    const f32x4 Z = {0.f, 0.f, 0.f, 0.f};
    const int woff = g * 8192 + (wv & 1) * 512 + (wv >> 1) * 128 + l * 8;
    {
        f32x4 re[4], im[4];
#pragma unroll
        for (int i = 0; i < 4; ++i) { re[i] = mfma(C.Aa[0], fin[i], Z); im[i] = mfma(C.Aa[1], fin[i], Z); }
#pragma unroll
        for (int r = 0; r < 4; ++r)
#pragma unroll
            for (int i0 = 0; i0 < 2; ++i0)
                *(LAS u32x2*)(s0 + woff + r * 2048 + i0 * 1024) = u32x2{pk(re[i0][r], im[i0][r]), pk(re[2 + i0][r], im[2 + i0][r])};
    }
    __syncthreads();
    u32x4 f[4];
    lds_read_frags(s0, wv, lane, f);
    f32x4 zr[2][2], zi[2][2];
    const u32x4 nR1[2] = {neg4(C.R1[0]), neg4(C.R1[1])};
#pragma unroll
    for (int ke = 0; ke < 2; ++ke) {
        u32x4 y[2];
#pragma unroll
        for (int ta = 0; ta < 2; ++ta) {
            f32x4 re = mfma(f[2 * ke + ta], ke ? C.C1[0] : C.E0[0], Z), im = mfma(f[2 * ke + ta], ke ? C.C1[1] : C.E0[1], Z);
            twid(re, im, C.tc[ta][ke], C.ts[ta][ke]);
            y[ta] = pack4(re, im);
        }
        const f32x4 pr = mfma(C.E0[0], y[0], Z), pi = mfma(C.E0[1], y[0], Z);
        zr[0][ke] = mfma(C.R1[0], y[1], pr); zi[0][ke] = mfma(C.R1[1], y[1], pi);
        zr[1][ke] = mfma(nR1[0], y[1], pr);  zi[1][ke] = mfma(nR1[1], y[1], pi);
    }
    const bool special = (q == 0) && (wv == 0);
    float s00 = zr[0][0][0] * C.sp[0][0], s01 = zi[0][0][0] * C.sp[0][1], s10 = zr[1][0][0] * C.sp[1][0], s11 = zi[1][0][0] * C.sp[1][1];
    if (special) mirror(sx, l, g, zr, zi, 1.0f, 0.25f);
#pragma unroll
    for (int o = 0; o < 2; ++o)
#pragma unroll
        for (int ke = 0; ke < 2; ++ke)
#pragma unroll
            for (int r = 0; r < 4; ++r) {
                float a = zr[o][ke][r], b = zi[o][ke][r], c = C.wr[o][ke][r], d = C.wi[o][ke][r];
                zr[o][ke][r] = a * c - b * d; zi[o][ke][r] = a * d + b * c;
            }
    if (special) {
        mirror(sx, l, g, zr, zi, -1.0f, 1.0f);
        if (lane == 0) { zr[0][0][0] = s00; zi[0][0][0] = s01; zr[1][0][0] = s10; zi[1][0][0] = s11; }
    }
    const u32x4 nC1[2] = {neg4(C.C1[0]), neg4(C.C1[1])};
    f32x4 ua[2][2], ub[2][2];
#pragma unroll
    for (int ke = 0; ke < 2; ++ke) {
        const u32x4 y0 = pack4(zi[0][ke], zr[0][ke]), y1 = pack4(zi[1][ke], zr[1][ke]);
#pragma unroll
        for (int ta = 0; ta < 2; ++ta) {
            f32x4 a = mfma(y0, ta ? C.C1[0] : C.E0[0], Z), b = mfma(y0, ta ? C.C1[1] : C.E0[1], Z);
            a = mfma(y1, ta ? nC1[0] : C.E0[0], a); b = mfma(y1, ta ? nC1[1] : C.E0[1], b);
            twid(a, b, C.tc[ke][ta], C.ts[ke][ta]);
            const u32x4 v = pack4(a, b);
            ua[ta][ke] = mfma(ke ? C.R1[0] : C.E0[0], v, Z); ub[ta][ke] = mfma(ke ? C.R1[1] : C.E0[1], v, Z);
        }
    }
#pragma unroll
    for (int r = 0; r < 4; ++r)
#pragma unroll
        for (int ta = 0; ta < 2; ++ta)
            *(LAS u32x2*)(s1 + woff + r * 2048 + ta * 1024) = u32x2{pk(ub[ta][0][r], ua[ta][0][r]), pk(ub[ta][1][r], ua[ta][1][r])};
    __syncthreads();
    lds_read_frags(s1, wv, lane, f);
    f32x4 re[4], im[4];
#pragma unroll
    for (int i = 0; i < 4; ++i) { re[i] = mfma(C.Ai[0], f[i], Z); im[i] = mfma(C.Ai[1], f[i], Z); }
    xstore(A.ws + WS_BUF2, h, q, wv, l, g, re, im);
}

struct P3Const { u32x4 A2[2], A1[2]; };
DI void p3_const(int wv, int lane, P3Const& C) {
    const int l = lane & 15, g = lane >> 4;
#pragma unroll
    for (int t = 0; t < 2; ++t)
#pragma unroll
        for (int k = 0; k < 4; ++k) {
            const int m = 4 * g + k, z = m >> 3, J = 8 * (2 * wv + z), b = 4 * t + (l >> 2);
            unsigned v = pk(coef8(J, m & 7, 0, b, l & 1, 0), coef8(J, m & 7, 1, b, l & 1, 0));
            C.A2[t][k] = ((l >> 1) & 1) == z ? v : 0u;
            C.A1[t][k] = pk(coef1(m, 0, 16 * t + l), coef1(m, 1, 16 * t + l));
        }
}
DI void p3_run(const Args& A, int h, int q, int wv, int lane, const u32x4 (&fin)[4], const P3Const& C, LAS char* s) {
    const int l = lane & 15, g = lane >> 4;
    const f32x4 Z = {0.f, 0.f, 0.f, 0.f};
    {
        LAS char* b = s + g * 4096 + (wv & 1) * 1024 + (wv >> 1) * 256 + (l ^ ((wv >> 1) & 1)) * 8;
#pragma unroll
        for (int i = 0; i < 4; ++i)
#pragma unroll
            for (int t = 0; t < 2; ++t) {
                f32x4 d = mfma(C.A2[t], fin[i], Z);
                *(LAS u32x2*)(b + t * 16384 + (i & 1) * 2048 + (i >> 1) * 128) = u32x2{pk(d[0], d[1]), pk(d[2], d[3])};
            }
    }
    __syncthreads();
    f32x4 d[2][4];
#pragma unroll
    for (int i = 0; i < 4; ++i) {
        LAS char* b = s + wv * 4096 + lane * 16 + (((i >> 1) ^ (g & 1)) * 8) + (i & 1) * 2048;
        u32x2 a = *(LAS u32x2*)b, c = *(LAS u32x2*)(b + 1024);
        u32x4 f = {a[0], a[1], c[0], c[1]};
        d[0][i] = mfma(C.A1[0], f, Z); d[1][i] = mfma(C.A1[1], f, Z);
    }
    const int c = (q >> 1) & 7, dt = 8 * (q & 1) + (q >> 4);
    GAS char* op = (GAS char*)A.out + ((size_t)(256 * g + 8 * wv + c) * 2048 + 1024 * h + 64 * dt + 4 * l) * 2;
#pragma unroll
    for (int t = 0; t < 2; ++t)
#pragma unroll
        for (int r = 0; r < 4; ++r)
            *(GAS u32x2*)(op + (size_t)(16 * t + r) * 64 * 4096) = u32x2{pk(d[t][0][r], d[t][1][r]), pk(d[t][2][r], d[t][3][r])};
}

#define IDS const int h = blockIdx.x >> 7, q = blockIdx.x & 127, lane = threadIdx.x & 63, \
                      wv = __builtin_amdgcn_readfirstlane(threadIdx.x >> 6), l = lane & 15, g = lane >> 4;

#define RLX_AGENT __ATOMIC_RELAXED, __HIP_MEMORY_SCOPE_AGENT
DI void publish(char* flags, int h, int q, int wv, int lane, u64 tag) {
    asm volatile("s_waitcnt vmcnt(0)" ::: "memory");
    if (lane < 16) __hip_atomic_store((GAS u64*)(flags + ((h * 128 + 16 * wv + lane) * 128 + q) * 8), tag, RLX_AGENT);
}
DI void await(char* flags, int h, int q, int wv, int lane, u64 tag) {
    GAS u64* p = (GAS u64*)(flags + ((h * 128 + q) * 128 + 16 * wv + (lane & 15)) * 8);
    for (unsigned spins = 0; spins < (1u << 20); ++spins) {
        if (__all(__hip_atomic_load(p, RLX_AGENT) == tag)) break;
        __builtin_amdgcn_s_sleep(1);
    }
    __builtin_amdgcn_fence(__ATOMIC_ACQUIRE, "wavefront");
}
__global__ void __launch_bounds__(512, 2) kf(Args A) {
    __shared__ __attribute__((aligned(16))) char sm[65536 + 8192];
    IDS
    GAS u64* ep = (GAS u64*)(A.ws + WS_EPOCH + 256 * h);
    const u64 e0 = __hip_atomic_load(ep, RLX_AGENT);
    P1In in; p1_load(A, h, q, wv, l, g, in);
    WIn w; w_load(A, h, q, wv, l, g, w);
    const u64 tag = e0 * 0x9E3779B97F4A7C15ull + 0x632BE59BD9B4E019ull;
    asm volatile("" :: "v"(tag));
    p1_run(A, h, q, wv, lane, in, (LAS char*)sm + 32768);
    publish(A.ws + WS_FLAG1, h, q, wv, lane, tag);
    P2Const C; p2_const(q, wv, lane, w, C);
    await(A.ws + WS_FLAG1, h, q, wv, lane, tag);
    u32x4 f[4]; xload(A.ws + WS_BUF1, h, q, wv, l, g, f);
    p2_run(A, h, q, wv, lane, f, C, (LAS char*)sm, (LAS char*)sm + 32768, (LAS char*)sm + 65536);
    publish(A.ws + WS_FLAG2, h, q, wv, lane, tag);
    if (q == 1 && threadIdx.x == 0) __hip_atomic_store(ep, e0 + 1, RLX_AGENT);
    P3Const C3; p3_const(wv, lane, C3);
    await(A.ws + WS_FLAG2, h, q, wv, lane, tag);
    xload(A.ws + WS_BUF2, h, q, wv, l, g, f);
    p3_run(A, h, q, wv, lane, f, C3, (LAS char*)sm);
}

extern "C" void kernel_launch(void* const* d_in, const int* in_sizes, int n_in, void* d_out, int out_size, void* d_ws, size_t ws_size,
                              hipStream_t stream) {
    Args A;
    A.x[0] = (const uint16_t*)d_in[0]; A.wr[0] = (const uint16_t*)d_in[1]; A.wi[0] = (const uint16_t*)d_in[2];
    A.x[1] = (const uint16_t*)d_in[3]; A.wr[1] = (const uint16_t*)d_in[4]; A.wi[1] = (const uint16_t*)d_in[5];
    A.out = (uint16_t*)d_out; A.ws = (char*)d_ws;
    kf<<<256, 512, 0, stream>>>(A);
}
```

```cpp
#include <hip/hip_runtime.h>
#include <stdint.h>

struct Srd { const void* p; unsigned bytes, fmt; };
struct Head { Srd x, out, ws; const void* wr; const void* wi; };
struct Args { Head h[2]; };
constexpr unsigned WS_HEAD = (4u << 20) + (512u << 10);
constexpr size_t WS_OFF = (size_t)0 << 20;

__global__ void __launch_bounds__(512, 2) gfnet2_kernel(Args) {
    __shared__ __attribute__((aligned(16))) char smem[132096];
    asm volatile(
        "s_lshr_b32 s20, s2, 7\n"
        "s_and_b32 s21, s2, 127\n"
        "s_lshl_b32 s26, s20, 6\n"
        "s_load_dwordx16 s[4:19], s[0:1], s26\n"
        "s_cbranch_scc0 .Lgf_pf\n"
        "s_getpc_b64 s[70:71]\n"
        ".Lgf_pc:\n"
        "v_lshlrev_b32 v192, 6, v0\n"
        "v_cmp_gt_u32 vcc, .Lgf_end-.Lgf_pc-4, v192\n"
        "s_cbranch_vccz .Lgf_pf\n"
        "v_min_u32 v192, .Lgf_end-.Lgf_pc-4, v192\n"
        "global_load_dword v192, v192, s[70:71]\n"
        ".Lgf_pf:\n"
        "s_bfe_u32 s27, s21, 0x30001\n"
        "s_lshl_b32 s27, s27, 11\n"
        "s_and_b32 s28, s21, 1\n"
        "s_lshl_b32 s28, s28, 10\n"
        "s_and_b32 s29, s21, 0x70\n"
        "s_lshl_b32 s29, s29, 3\n"
        "s_or_b32 s28, s28, s29\n"
        "s_or_b32 s28, s28, s27\n"
        "v_and_b32 v112, 15, v0\n"
        "v_bfe_u32 v113, v0, 4, 2\n"
        "v_lshrrev_b32 v144, 6, v0\n"
        "v_lshl_add_u32 v13, v113, 20, s28\n"
        "v_readfirstlane_b32 s22, v144\n"
        "v_lshl_add_u32 v13, v112, 3, v13\n"
        "v_lshl_add_u32 v13, v144, 14, v13\n"
        "s_mov_b32 s36, 0x20000\n"
        "s_mov_b32 s37, 0x40000\n"
        "s_mov_b32 s38, 0x60000\n"
        "s_mov_b32 s39, 0x80000\n"
        "s_mov_b32 s40, 0xa0000\n"
        "s_mov_b32 s41, 0xc0000\n"
        "s_mov_b32 s42, 0xe0000\n"
        "s_mov_b32 s56, 0x440000\n"
        "s_cmp_lg_u32 s20, 0\n"
        "s_cbranch_scc1 .Lgf_x0\n"
        "s_waitcnt lgkmcnt(0)\n"
        "buffer_load_dwordx2 v[224:225], v13, s[4:7], 0 offen nt\n"
        "buffer_load_dwordx2 v[226:227], v13, s[4:7], s36 offen nt\n"
        "buffer_load_dwordx2 v[228:229], v13, s[4:7], s37 offen nt\n"
        "buffer_load_dwordx2 v[230:231], v13, s[4:7], s38 offen nt\n"
        ".Lgf_x0:\n"
        "v_and_b32 v1, 63, v0\n"
        "v_lshlrev_b32 v1, 4, v1\n"
        "s_lshr_b32 s29, s22, 2\n"
        "s_and_b32 s30, s22, 3\n"
        "s_lshl_b32 s31, s29, 4\n"
        "s_lshl_b32 s34, s29, 8\n"
        "s_add_u32 s34, s34, s30\n"
        "s_lshl_b32 s34, s34, 2\n"
        "v_lshl_add_u32 v144, v113, 2, s30\n"
        "v_add_u32 v145, s31, v112\n"
        "v_add_u32 v146, s34, v1\n"
        "v_cmp_eq_u32 vcc, 1, v145\n"
        "v_lshrrev_b32 v147, 1, v145\n"
        "v_and_b32 v148, 1, v112\n"
        "v_lshlrev_b32 v148, 3, v148\n"
        "v_cndmask_b32_e64 v147, v147, 16, vcc\n"
        "v_cndmask_b32_e64 v148, v148, 0, vcc\n"
        "v_lshlrev_b32 v149, 1, v144\n"
        "v_cmp_eq_u32 vcc, 0, v144\n"
        "v_mad_u32_u24 v148, v149, v147, v148\n"
        "v_add_u32 v147, v148, v147\n"
        "v_mul_u32_u24 v149, v145, v144\n"
        "v_add_u32 v150, 8, v149\n"
        "v_lshlrev_b32 v151, 4, v112\n"
        "v_cndmask_b32 v150, v150, v151, vcc\n"
        "v_cvt_f32_u32 v148, v148\n"
        "v_cvt_f32_u32 v147, v147\n"
        "v_cvt_f32_u32 v149, v149\n"
        "v_cvt_f32_u32 v150, v150\n"
        "v_mul_f32 v148, 0x3d000000, v148\n"
        "v_mul_f32 v147, 0x3d000000, v147\n"
        "v_mul_f32 v149, 0x3d000000, v149\n"
        "v_mul_f32 v150, 0x3d000000, v150\n"
        "v_cos_f32 v148, v148\n"
        "v_cos_f32 v147, v147\n"
        "v_cos_f32 v149, v149\n"
        "v_cos_f32 v150, v150\n"
        "s_lshr_b32 s35, s21, 3\n"
        "s_and_b32 s61, s21, 7\n"
        "s_cmp_lg_u32 s35, 0\n"
        "s_cselect_b32 s74, 5, 4\n"
        "s_lshl_b32 s61, s61, s74\n"
        "s_add_u32 s23, s35, s61\n"
        "v_cvt_pk_bf16_f32 v148, v148, v147\n"
        "v_cvt_pk_bf16_f32 v149, v149, v150\n"
        "ds_write_b32 v146, v148 offset:20480\n"
        "ds_write_b32 v146, v149 offset:22528\n"
        "s_cmp_lg_u32 s23, 0\n"
        "s_cselect_b32 s74, 8, 7\n"
        "s_cmp_lg_u32 s29, 0\n"
        "s_cselect_b64 vcc, -1, 0\n"
        "s_cselect_b32 s35, 0xba000000, s29\n"
        "s_cselect_b32 s61, 0x35800000, 1.0\n"
        "v_cndmask_b32 v147, v112, v144, vcc\n"
        "v_cndmask_b32 v148, v144, v112, vcc\n"
        "s_or_b32 s35, s35, 0x3a000000\n"
        "v_and_b32 v150, v144, v112\n"
        "v_lshrrev_b32 v147, 1, v147\n"
        "v_lshrrev_b32 v148, 1, v148\n"
        "v_and_b32 v150, 1, v150\n"
        "v_lshlrev_b32 v149, s74, v147\n"
        "v_lshlrev_b32 v150, 10, v150\n"
        "v_add_u32 v149, s23, v149\n"
        "v_mad_u32_u24 v149, v148, v149, v150\n"
        "v_cvt_f32_u32 v149, v149\n"
        "v_mul_f32 v149, s35, v149\n"
        "v_cos_f32 v150, v149\n"
        "v_sin_f32 v151, v149\n"
        "s_lshl_b32 s24, s22, s74\n"
        "s_add_u32 s24, s24, s23\n"
        "s_sub_u32 s25, 0x800, s24\n"
        "s_and_b32 s25, s25, 0x7ff\n"
        "v_mul_f32 v150, s61, v150\n"
        "v_mul_f32 v151, s61, v151\n"
        "v_cvt_pk_bf16_f32 v152, v150, v151\n"
        "v_cvt_pk_bf16_f32 v153, -v151, v150\n"
        "s_cmp_eq_u32 s23, 0\n"
        "s_cbranch_scc1 .Lgf_aa\n"
        ".Lgf_aa_ret:\n"
        "ds_write2st64_b32 v146, v152, v153 offset0:64 offset1:72\n"
        "s_bfe_u32 s35, s22, 0x10001\n"
        "s_and_b32 s61, s22, 1\n"
        "s_cmp_eq_u32 s22, 3\n"
        "s_cselect_b32 s35, 0, s35\n"
        "s_cselect_b32 s74, 0.5, 0\n"
        "s_lshl_b32 s34, s22, 11\n"
        "v_lshl_add_u32 v147, v112, 1, s35\n"
        "v_lshl_add_u32 v148, v113, 3, s61\n"
        "s_mov_b32 s35, 0x3c800000\n"
        "s_cmp_lg_u32 s29, 0\n"
        "s_cmov_b32 s35, 0x3a000000\n"
        "v_cvt_f32_u32 v147, v147\n"
        "v_cvt_f32_u32 v148, v148\n"
        "v_add_u32 v146, s34, v1\n"
        "v_mul_f32 v147, s35, v147\n"
        "v_fma_f32 v148, v148, v147, s74\n"
        "v_fma_f32 v149, v147, 2.0, v148\n"
        "v_fma_f32 v150, v147, 4.0, v148\n"
        "v_fma_f32 v151, v147, 4.0, v149\n"
        "v_cos_f32 v152, v148\n"
        "v_sin_f32 v156, v148\n"
        "v_cos_f32 v153, v149\n"
        "v_sin_f32 v157, v149\n"
        "v_cos_f32 v154, v150\n"
        "v_sin_f32 v158, v150\n"
        "v_cos_f32 v155, v151\n"
        "v_sin_f32 v159, v151\n"
        "s_cbranch_scc1 .Lgf_shear\n"
        "v_cvt_pk_bf16_f32 v160, v152, v156\n"
        "v_cvt_pk_bf16_f32 v164, -v156, v152\n"
        "v_cvt_pk_bf16_f32 v161, v153, v157\n"
        "v_cvt_pk_bf16_f32 v165, -v157, v153\n"
        "v_cvt_pk_bf16_f32 v162, v154, v158\n"
        "v_cvt_pk_bf16_f32 v166, -v158, v154\n"
        "v_cvt_pk_bf16_f32 v163, v155, v159\n"
        "v_cvt_pk_bf16_f32 v167, -v159, v155\n"
        "s_branch .Lgf_tabw\n"
        ".Lgf_shear:\n"
        "v_rcp_f32 v148, v152\n"
        "v_mul_f32 v152, -2.0, v152\n"
        "v_rcp_f32 v149, v153\n"
        "v_mul_f32 v153, -2.0, v153\n"
        "v_rcp_f32 v150, v154\n"
        "v_mul_f32 v154, -2.0, v154\n"
        "v_rcp_f32 v151, v155\n"
        "v_mul_f32 v155, -2.0, v155\n"
        "v_mul_f32 v160, v156, v148\n"
        "v_mul_f32 v164, v156, v152\n"
        "v_mul_f32 v161, v157, v149\n"
        "v_mul_f32 v165, v157, v153\n"
        "v_mul_f32 v162, v158, v150\n"
        "v_mul_f32 v166, v158, v154\n"
        "v_mul_f32 v163, v159, v151\n"
        "v_mul_f32 v167, v159, v155\n"
        ".Lgf_tabw:\n"
        "ds_write_b128 v146, v[160:163] offset:0\n"
        "ds_write_b128 v146, v[164:167] offset:1024\n"
        "s_cmp_lg_u32 s20, 0\n"
        "s_cbranch_scc1 .Lgf_x2\n"
        "buffer_load_dwordx2 v[232:233], v13, s[4:7], s39 offen nt\n"
        "buffer_load_dwordx2 v[234:235], v13, s[4:7], s40 offen nt\n"
        "buffer_load_dwordx2 v[236:237], v13, s[4:7], s41 offen nt\n"
        "buffer_load_dwordx2 v[238:239], v13, s[4:7], s42 offen nt\n"
        ".Lgf_x2:\n"
        "s_cmp_eq_u32 s20, 0\n"
        "s_cbranch_scc1 .Lgf_x1\n"
        "s_sleep 8\n"
        "s_waitcnt lgkmcnt(0)\n"
        "buffer_load_dwordx2 v[224:225], v13, s[4:7], 0 offen nt\n"
        "buffer_load_dwordx2 v[226:227], v13, s[4:7], s36 offen nt\n"
        "buffer_load_dwordx2 v[228:229], v13, s[4:7], s37 offen nt\n"
        "buffer_load_dwordx2 v[230:231], v13, s[4:7], s38 offen nt\n"
        "buffer_load_dwordx2 v[232:233], v13, s[4:7], s39 offen nt\n"
        "buffer_load_dwordx2 v[234:235], v13, s[4:7], s40 offen nt\n"
        "buffer_load_dwordx2 v[236:237], v13, s[4:7], s41 offen nt\n"
        "buffer_load_dwordx2 v[238:239], v13, s[4:7], s42 offen nt\n"
        ".Lgf_x1:\n"
        "buffer_load_dwordx2 v[6:7], off, s[12:15], s56 sc1\n"
        "s_lshl_b32 s35, s22, 4\n"
        "s_mov_b32 s62, 0x00ff00ff\n"
        "s_mov_b32 s63, 0x00ff00ff\n"
        "s_cmp_eq_u32 s22, 0\n"
        "s_cselect_b64 s[64:65], s[62:63], 0\n"
        "v_and_b32 v147, 7, v112\n"
        "v_and_or_b32 v148, v112, 8, s35\n"
        "v_cndmask_b32_e64 v149, 8, 7, s[64:65]\n"
        "v_lshl_add_u32 v148, v147, v149, v148\n"
        "v_cvt_f32_u32 v149, v113\n"
        "v_cvt_f32_u32 v148, v148\n"
        "v_add_f32 v149, v149, v149\n"
        "v_mul_f32 v148, 0x3a000000, v148\n"
        "v_mul_f32 v149, v149, v148\n"
        "v_add_f32 v150, v149, v148\n"
        "v_cos_f32 v152, v149\n"
        "v_sin_f32 v154, v149\n"
        "v_cos_f32 v153, v150\n"
        "v_sin_f32 v155, v150\n"
        "v_cvt_pk_bf16_f32 v156, v152, v154\n"
        "v_cvt_pk_bf16_f32 v157, -v154, v152\n"
        "v_cvt_pk_bf16_f32 v158, v153, v155\n"
        "v_cvt_pk_bf16_f32 v159, -v155, v153\n"
        "s_cbranch_scc1 .Lgf_a2\n"
        ".Lgf_a2_ret:\n"
        "v_cndmask_b32_e64 v32, 0, v156, s[62:63]\n"
        "v_cndmask_b32_e64 v33, v156, 0, s[62:63]\n"
        "v_cndmask_b32_e64 v34, 0, v158, s[62:63]\n"
        "v_cndmask_b32_e64 v35, v158, 0, s[62:63]\n"
        "v_cndmask_b32_e64 v36, 0, v157, s[62:63]\n"
        "v_cndmask_b32_e64 v37, v157, 0, s[62:63]\n"
        "v_cndmask_b32_e64 v38, 0, v159, s[62:63]\n"
        "v_cndmask_b32_e64 v39, v159, 0, s[62:63]\n"
        "s_lshl_b32 s34, s22, 12\n"
        "s_add_u32 s34, s34, 99328\n"
        "v_add_u32 v144, s34, v1\n"
        "ds_write_b128 v144, v[32:35]\n"
        "ds_write_b128 v144, v[36:39] offset:2048\n"
        "v_and_b32 v19, 0x3c0, v1\n"
        "v_and_b32 v144, 2, v112\n"
        "v_lshl_add_u32 v19, v144, 3, v19\n"
        "v_and_b32 v144, 1, v112\n"
        "v_lshl_add_u32 v19, v144, 11, v19\n"
        "v_add_u32 v144, s34, v19\n"
        "ds_read_b128 v[32:35], v144\n"
        "ds_read_b128 v[36:39], v144 offset:32\n"
        "s_mul_i32 s24, s24, 1026\n"
        "s_mul_i32 s25, s25, 1026\n"
        "s_or_b32 s60, s21, s22\n"
        "v_add_u32 v14, s24, v1\n"
        "v_add3_u32 v15, v1, 2, s25\n"
        "s_and_b32 s34, s24, 2\n"
        "s_cselect_b32 s61, 0, 0x400\n"
        "s_add_u32 s26, s24, s61\n"
        "s_add_u32 s61, s25, s61\n"
        "v_add_u32 v14, s34, v14\n"
        "v_and_b32 v15, -4, v15\n"
        "v_mov_b32 v11, s26\n"
        "v_mov_b32 v12, s61\n"
        "s_waitcnt lgkmcnt(0)\n"
        "s_barrier\n"
        "ds_read_b128 v[24:27], v1 offset:20480\n"
        "ds_read_b128 v[28:31], v1 offset:21504\n"
        "s_and_b32 s61, s22, 1\n"
        "s_lshr_b32 s74, s22, 1\n"
        "s_lshl_b32 s35, s61, 2\n"
        "s_add_u32 s35, s35, s74\n"
        "s_lshl_b32 s35, s35, 7\n"
        "s_add_u32 s34, s35, 66560\n"
        "v_lshl_add_u32 v10, v113, 12, s34\n"
        "v_and_b32 v144, 8, v112\n"
        "v_lshl_add_u32 v10, v144, 8, v10\n"
        "v_lshl_add_u32 v10, v147, 4, v10\n"
        "s_lshl_b32 s31, s22, 12\n"
        "s_add_u32 s31, s31, 33792\n"
        "v_lshrrev_b32 v8, 1, v1\n"
        "v_add_u32 v8, s31, v8\n"
        "s_mov_b32 s50, 0x05040100\n"
        "s_mov_b32 s51, 0x07060302\n"
        "s_lshl_b32 s34, s21, 8\n"
        "v_lshrrev_b32 v144, 4, v0\n"
        "v_lshl_add_u32 v2, v112, 4, s34\n"
        "v_lshl_add_u32 v2, v144, 17, v2\n"
        "s_mov_b32 s47, 0x8000\n"
        "s_mov_b32 s48, 0x10000\n"
        "s_mov_b32 s49, 0x18000\n"
        "s_cmp_eq_u32 s60, 0\n"
        "s_cbranch_scc1 .Lgf_spl\n"
        "s_waitcnt vmcnt(7)\n"
        "v_perm_b32 v176, v226, v224, s50\n"
        "v_perm_b32 v184, v227, v225, s50\n"
        "v_perm_b32 v180, v226, v224, s51\n"
        "v_perm_b32 v188, v227, v225, s51\n"
        "s_waitcnt vmcnt(5)\n"
        "v_perm_b32 v177, v230, v228, s50\n"
        "v_perm_b32 v185, v231, v229, s50\n"
        "v_perm_b32 v181, v230, v228, s51\n"
        "v_perm_b32 v189, v231, v229, s51\n"
        "s_waitcnt vmcnt(3)\n"
        "v_perm_b32 v178, v234, v232, s50\n"
        "v_perm_b32 v186, v235, v233, s50\n"
        "v_perm_b32 v182, v234, v232, s51\n"
        "v_perm_b32 v190, v235, v233, s51\n"
        "s_waitcnt vmcnt(1)\n"
        "v_perm_b32 v179, v238, v236, s50\n"
        "v_perm_b32 v187, v239, v237, s50\n"
        "v_perm_b32 v183, v238, v236, s51\n"
        "v_perm_b32 v191, v239, v237, s51\n"
        ".Lgf_spl_ret:\n"
        "s_waitcnt lgkmcnt(0)\n"
        "v_mfma_f32_16x16x32_bf16 v[192:195], v[24:27], v[176:179], 0\n"
        "s_nop 1\n"
        "v_mfma_f32_16x16x32_bf16 v[200:203], v[24:27], v[184:187], 0\n"
        "s_nop 1\n"
        "v_mfma_f32_16x16x32_bf16 v[208:211], v[28:31], v[176:179], 0\n"
        "s_nop 1\n"
        "v_mfma_f32_16x16x32_bf16 v[216:219], v[28:31], v[184:187], 0\n"
        "v_cvt_pk_bf16_f32 v240, v192, v193\n"
        "v_cvt_pk_bf16_f32 v241, v194, v195\n"
        "v_mfma_f32_16x16x32_bf16 v[196:199], v[24:27], v[180:183], 0\n"
        "v_cvt_pk_bf16_f32 v242, v200, v201\n"
        "v_cvt_pk_bf16_f32 v243, v202, v203\n"
        "v_mfma_f32_16x16x32_bf16 v[204:207], v[24:27], v[188:191], 0\n"
        "v_cvt_pk_bf16_f32 v248, v208, v209\n"
        "v_cvt_pk_bf16_f32 v249, v210, v211\n"
        "ds_write_b128 v10, v[240:243] offset:0\n"
        "v_mfma_f32_16x16x32_bf16 v[212:215], v[28:31], v[180:183], 0\n"
        "v_cvt_pk_bf16_f32 v250, v216, v217\n"
        "v_cvt_pk_bf16_f32 v251, v218, v219\n"
        "v_mfma_f32_16x16x32_bf16 v[220:223], v[28:31], v[188:191], 0\n"
        "v_cvt_pk_bf16_f32 v244, v196, v197\n"
        "v_cvt_pk_bf16_f32 v245, v198, v199\n"
        "ds_write_b128 v10, v[248:251] offset:16384\n"
        "v_cvt_pk_bf16_f32 v246, v204, v205\n"
        "v_cvt_pk_bf16_f32 v247, v206, v207\n"
        "ds_write_b128 v10, v[244:247] offset:1024\n"
        "v_cvt_pk_bf16_f32 v252, v212, v213\n"
        "v_cvt_pk_bf16_f32 v253, v214, v215\n"
        "v_cvt_pk_bf16_f32 v254, v220, v221\n"
        "v_cvt_pk_bf16_f32 v255, v222, v223\n"
        "ds_write_b128 v10, v[252:255] offset:17408\n"
        "s_cbranch_scc1 .Lgf_wx\n"
        "global_load_dwordx4 v[160:163], v14, s[16:17] nt\n"
        "global_load_dwordx4 v[164:167], v14, s[18:19] nt\n"
        "global_load_ushort v22, v11, s[16:17]\n"
        "global_load_ushort v23, v11, s[18:19]\n"
        ".Lgf_wx:\n"
        "s_waitcnt lgkmcnt(0)\n"
        "s_barrier\n"
        "ds_read_b64 v[176:177], v8 offset:32768\n"
        "ds_read_b64 v[178:179], v8 offset:33280\n"
        "ds_read_b64 v[180:181], v8 offset:33792\n"
        "ds_read_b64 v[182:183], v8 offset:34304\n"
        "ds_read_b64 v[184:185], v8 offset:34816\n"
        "ds_read_b64 v[186:187], v8 offset:35328\n"
        "ds_read_b64 v[188:189], v8 offset:35840\n"
        "ds_read_b64 v[190:191], v8 offset:36352\n"
        "s_waitcnt lgkmcnt(6)\n"
        "v_mfma_f32_16x16x32_bf16 v[192:195], v[32:35], v[176:179], 0\n"
        "s_waitcnt lgkmcnt(4)\n"
        "v_mfma_f32_16x16x32_bf16 v[196:199], v[32:35], v[180:183], 0\n"
        "s_waitcnt lgkmcnt(2)\n"
        "v_mfma_f32_16x16x32_bf16 v[200:203], v[32:35], v[184:187], 0\n"
        "s_waitcnt lgkmcnt(0)\n"
        "v_mfma_f32_16x16x32_bf16 v[204:207], v[32:35], v[188:191], 0\n"
        "v_mfma_f32_16x16x32_bf16 v[208:211], v[36:39], v[176:179], 0\n"
        "s_nop 0\n"
        "v_cvt_pk_bf16_f32 v224, v192, v193\n"
        "v_mfma_f32_16x16x32_bf16 v[212:215], v[36:39], v[180:183], 0\n"
        "v_cvt_pk_bf16_f32 v225, v196, v197\n"
        "v_cvt_pk_bf16_f32 v228, v194, v195\n"
        "v_mfma_f32_16x16x32_bf16 v[216:219], v[36:39], v[184:187], 0\n"
        "v_cvt_pk_bf16_f32 v226, v200, v201\n"
        "v_cvt_pk_bf16_f32 v227, v204, v205\n"
        "buffer_store_dwordx4 v[224:227], v2, s[12:15], 0 offen sc1\n"
        "v_mfma_f32_16x16x32_bf16 v[220:223], v[36:39], v[188:191], 0\n"
        "v_cvt_pk_bf16_f32 v229, v198, v199\n"
        "v_cvt_pk_bf16_f32 v230, v202, v203\n"
        "v_cvt_pk_bf16_f32 v231, v206, v207\n"
        "buffer_store_dwordx4 v[228:231], v2, s[12:15], s47 offen sc1\n"
        "v_cvt_pk_bf16_f32 v232, v208, v209\n"
        "v_cvt_pk_bf16_f32 v233, v212, v213\n"
        "v_cvt_pk_bf16_f32 v234, v216, v217\n"
        "v_cvt_pk_bf16_f32 v236, v210, v211\n"
        "v_cvt_pk_bf16_f32 v235, v220, v221\n"
        "buffer_store_dwordx4 v[232:235], v2, s[12:15], s48 offen sc1\n"
        "v_cvt_pk_bf16_f32 v237, v214, v215\n"
        "v_cvt_pk_bf16_f32 v238, v218, v219\n"
        "v_cvt_pk_bf16_f32 v239, v222, v223\n"
        "buffer_store_dwordx4 v[236:239], v2, s[12:15], s49 offen sc1\n"
        "s_waitcnt vmcnt(8)\n"
        "v_add_u32 v6, 0x9e3779b9, v6\n"
        "v_add_u32 v7, 0x85ebca6b, v7\n"
        "v_and_b32 v6, -2, v6\n"
        "s_lshl_b32 s34, s21, 15\n"
        "v_lshrrev_b32 v144, 4, v0\n"
        "v_lshl_add_u32 v3, v112, 4, s34\n"
        "v_lshl_add_u32 v3, v144, 10, v3\n"
        "s_lshl_b32 s34, s21, 3\n"
        "s_add_u32 s34, s34, 0x400000\n"
        "s_mul_i32 s61, s22, 4096\n"
        "s_add_u32 s34, s34, s61\n"
        "v_lshl_add_u32 v4, v1, 6, s34\n"
        "s_lshr_b32 s34, s21, 4\n"
        "s_mul_i32 s34, s34, 4\n"
        "s_and_b32 s61, s21, 3\n"
        "s_add_u32 s34, s34, s61\n"
        "s_lshl_b32 s34, s34, 3\n"
        "s_add_u32 s34, s34, s22\n"
        "s_lshl_b32 s34, s34, 7\n"
        "s_add_u32 s34, s34, 0x400000\n"
        "v_lshl_add_u32 v5, v112, 3, s34\n"
        "s_add_u32 s34, s35, 33792\n"
        "v_lshl_add_u32 v9, v113, 13, s34\n"
        "v_lshl_add_u32 v9, v112, 3, v9\n"
        "s_lshl_b32 s34, s22, 12\n"
        "s_add_u32 s34, s34, 99328\n"
        "v_lshlrev_b32 v144, 4, v113\n"
        "v_xor_b32 v16, v144, v1\n"
        "v_add_u32 v16, s34, v16\n"
        "v_lshrrev_b32 v144, 2, v112\n"
        "v_xor_b32 v144, v144, v113\n"
        "v_lshl_add_u32 v144, v112, 2, v144\n"
        "v_lshl_add_u32 v17, v144, 4, s34\n"
        "v_xor_b32 v144, 63, v144\n"
        "v_lshl_add_u32 v18, v144, 4, s34\n"
        "ds_read_b128 v[24:27], v1 offset:16384\n"
        "ds_read_b128 v[28:31], v1 offset:18432\n"
        "ds_read_b128 v[32:35], v19 offset:17408\n"
        "ds_read_b128 v[36:39], v19 offset:17440\n"
        "s_waitcnt vmcnt(0)\n"
        "s_mov_b64 exec, 0xf\n"
        "buffer_store_dwordx2 v[6:7], v4, s[12:15], 0 offen offset:0 sc1\n"
        "s_mov_b64 exec, -1\n"
        ".Lgf_pub0:\n"
        "s_cmp_eq_u32 s60, 0\n"
        "s_cbranch_scc1 .Lgf_ml\n"
        "global_load_dwordx4 v[168:171], v15, s[16:17] nt\n"
        "global_load_dwordx4 v[172:175], v15, s[18:19] nt\n"
        "global_load_ushort v20, v12, s[16:17]\n"
        "global_load_ushort v21, v12, s[18:19]\n"
        ".Lgf_ml:\n"
        "ds_read_b128 v[48:51], v1 offset:0\n"
        "ds_read_b128 v[52:55], v1 offset:1024\n"
        "ds_read_b128 v[56:59], v1 offset:2048\n"
        "ds_read_b128 v[60:63], v1 offset:3072\n"
        "ds_read_b128 v[64:67], v1 offset:4096\n"
        "ds_read_b128 v[68:71], v1 offset:5120\n"
        "ds_read_b128 v[80:83], v1 offset:8192\n"
        "ds_read_b128 v[84:87], v1 offset:9216\n"
        "ds_read_b128 v[88:91], v1 offset:10240\n"
        "ds_read_b128 v[92:95], v1 offset:11264\n"
        "ds_read_b128 v[96:99], v1 offset:12288\n"
        "ds_read_b128 v[100:103], v1 offset:13312\n"
        "ds_read_b128 v[104:107], v1 offset:14336\n"
        "ds_read_b128 v[108:111], v1 offset:15360\n"
        "v_mov_b32 v144, 0\n"
        "v_mov_b32 v145, 0\n"
        "v_mov_b32 v146, 0\n"
        "v_mov_b32 v147, 0\n"
        "v_mov_b32 v148, 0\n"
        "v_mov_b32 v149, 0\n"
        "v_mov_b32 v150, 0\n"
        "v_mov_b32 v151, 0\n"
        "v_mov_b32 v152, 0\n"
        "v_mov_b32 v153, 0\n"
        "v_mov_b32 v154, 0\n"
        "v_mov_b32 v155, 0\n"
        "v_mov_b32 v156, 0\n"
        "v_mov_b32 v157, 0\n"
        "v_mov_b32 v158, 0\n"
        "v_mov_b32 v159, 0\n"
        "s_mov_b32 exec_lo, 0x100001\n"
        "s_mov_b32 exec_hi, 0x10000100\n"
        "v_mov_b32 v144, 0x3f80\n"
        "v_mov_b32 v148, 1.0\n"
        "s_mov_b32 exec_lo, 0x8008000\n"
        "s_mov_b32 exec_hi, 0x80080\n"
        "v_mov_b32 v152, 1.0\n"
        "v_mov_b32 v156, 0x3f80\n"
        "s_mov_b32 exec_lo, 0x200002\n"
        "s_mov_b32 exec_hi, 0x20000200\n"
        "v_mov_b32 v145, 0x3f80\n"
        "v_mov_b32 v149, 1.0\n"
        "s_mov_b32 exec_lo, 0x4004000\n"
        "s_mov_b32 exec_hi, 0x40040\n"
        "v_mov_b32 v153, 1.0\n"
        "v_mov_b32 v157, 0x3f80\n"
        "s_mov_b32 exec_lo, 0x400004\n"
        "s_mov_b32 exec_hi, 0x40000400\n"
        "v_mov_b32 v146, 0x3f80\n"
        "v_mov_b32 v150, 1.0\n"
        "s_mov_b32 exec_lo, 0x2002000\n"
        "s_mov_b32 exec_hi, 0x20020\n"
        "v_mov_b32 v154, 1.0\n"
        "v_mov_b32 v158, 0x3f80\n"
        "s_mov_b32 exec_lo, 0x800008\n"
        "s_mov_b32 exec_hi, 0x80000800\n"
        "v_mov_b32 v147, 0x3f80\n"
        "v_mov_b32 v151, 1.0\n"
        "s_mov_b32 exec_lo, 0x1001000\n"
        "s_mov_b32 exec_hi, 0x10010\n"
        "v_mov_b32 v155, 1.0\n"
        "v_mov_b32 v159, 0x3f80\n"
        "s_mov_b64 exec, -1\n"
        "s_cmp_eq_u32 s60, 0\n"
        "s_cbranch_scc1 .Lgf_spb\n"
        ".Lgf_spb_ret:\n"
        "s_bitcmp1_b32 s24, 1\n"
        "s_cbranch_scc0 .Lgf_sd0\n"
        "v_readlane_b32 s52, v163, 63\n"
        "v_lshlrev_b32 v22, 16, v22\n"
        "v_readlane_b32 s53, v167, 63\n"
        "v_lshlrev_b32 v23, 16, v23\n"
        "s_lshr_b32 s52, s52, 16\n"
        "s_lshr_b32 s53, s53, 16\n"
        "v_mov_b32_dpp v22, v163 wave_shr:1 row_mask:0xf bank_mask:0xf\n"
        "v_alignbit_b32 v163, v163, v162, 16\n"
        "v_alignbit_b32 v162, v162, v161, 16\n"
        "v_alignbit_b32 v161, v161, v160, 16\n"
        "v_alignbit_b32 v160, v160, v22, 16\n"
        "v_mov_b32 v22, s52\n"
        "v_mov_b32_dpp v23, v167 wave_shr:1 row_mask:0xf bank_mask:0xf\n"
        "v_alignbit_b32 v167, v167, v166, 16\n"
        "v_alignbit_b32 v166, v166, v165, 16\n"
        "v_alignbit_b32 v165, v165, v164, 16\n"
        "v_alignbit_b32 v164, v164, v23, 16\n"
        "v_mov_b32 v23, s53\n"
        ".Lgf_sd0:\n"
        "s_cmp_eq_u32 s60, 0\n"
        "ds_write_b128 v16, v[160:163] offset:0\n"
        "ds_write_b128 v16, v[164:167] offset:1024\n"
        "ds_read_b128 v[160:163], v17 offset:0\n"
        "ds_read_b128 v[164:167], v17 offset:1024\n"
        "s_waitcnt lgkmcnt(0)\n"
        "v_mfma_f32_16x16x32_bf16 v[112:115], v[160:163], v[144:147], 0\n"
        "v_mfma_f32_16x16x32_bf16 v[120:123], v[160:163], v[148:151], 0\n"
        "v_mfma_f32_16x16x32_bf16 v[116:119], v[164:167], v[144:147], 0\n"
        "v_mfma_f32_16x16x32_bf16 v[124:127], v[164:167], v[148:151], 0\n"
        "v_or_b32 v6, 1, v6\n"
        "s_mov_b32 s57, 0x20000\n"
        "s_mov_b64 exec, 0xffff\n"
        ".Lgf_poll0:\n"
        "v_mov_b32 v176, 0\n"
        "v_mov_b32 v177, 0\n"
        "buffer_atomic_or_x2 v[176:177], v5, s[12:15], 0 offen offset:0 sc0 sc1\n"
        "s_sub_u32 s57, s57, 1\n"
        "s_cbranch_scc1 .Lgf_go0\n"
        "s_waitcnt vmcnt(0)\n"
        "v_or_b32 v176, 1, v176\n"
        "v_cmp_ne_u64 vcc, v[176:177], v[6:7]\n"
        "s_cbranch_vccnz .Lgf_poll0\n"
        ".Lgf_go0:\n"
        "s_mov_b64 exec, -1\n"
        "buffer_load_dwordx4 v[224:227], v3, s[12:15], 0 offen offset:0 sc1\n"
        "buffer_load_dwordx4 v[228:231], v3, s[12:15], 0 offen offset:256 sc1\n"
        "buffer_load_dwordx4 v[232:235], v3, s[12:15], 0 offen offset:512 sc1\n"
        "buffer_load_dwordx4 v[236:239], v3, s[12:15], 0 offen offset:768 sc1\n"
        "s_cmp_eq_u32 s60, 0\n"
        "s_bitcmp1_b32 s24, 1\n"
        "s_cbranch_scc1 .Lgf_sm2\n"
        "v_mov_b32_dpp v20, v168 wave_shl:1 row_mask:0xf bank_mask:0xf\n"
        "v_mov_b32 v19, v168\n"
        "v_alignbit_b32 v168, v169, v168, 16\n"
        "v_alignbit_b32 v169, v170, v169, 16\n"
        "v_alignbit_b32 v170, v171, v170, 16\n"
        "v_alignbit_b32 v171, v20, v171, 16\n"
        "v_mov_b32 v20, v19\n"
        "v_mov_b32_dpp v21, v172 wave_shl:1 row_mask:0xf bank_mask:0xf\n"
        "v_mov_b32 v19, v172\n"
        "v_alignbit_b32 v172, v173, v172, 16\n"
        "v_alignbit_b32 v173, v174, v173, 16\n"
        "v_alignbit_b32 v174, v175, v174, 16\n"
        "v_alignbit_b32 v175, v21, v175, 16\n"
        "v_mov_b32 v21, v19\n"
        ".Lgf_sm2:\n"
        "s_cmp_eq_u32 s60, 0\n"
        "ds_write_b128 v16, v[168:171] offset:2048\n"
        "ds_write_b128 v16, v[172:175] offset:3072\n"
        "ds_read_b128 v[168:171], v18 offset:2048\n"
        "ds_read_b128 v[172:175], v18 offset:3072\n"
        "s_waitcnt lgkmcnt(0)\n"
        "v_mfma_f32_16x16x32_bf16 v[128:131], v[168:171], v[152:155], 0\n"
        "v_mfma_f32_16x16x32_bf16 v[136:139], v[168:171], v[156:159], 0\n"
        "v_mfma_f32_16x16x32_bf16 v[132:135], v[172:175], v[152:155], 0\n"
        "v_mfma_f32_16x16x32_bf16 v[140:143], v[172:175], v[156:159], 0\n"
        "s_waitcnt lgkmcnt(0)\n"
        "v_lshlrev_b32 v20, 16, v20\n"
        "v_lshlrev_b32 v21, 16, v21\n"
        "v_lshlrev_b32 v22, 16, v22\n"
        "v_lshlrev_b32 v23, 16, v23\n"
        "s_mov_b64 exec, 1\n"
        "v_add_f32 v112, v112, v20\n"
        "v_sub_f32 v116, v116, v21\n"
        "v_add_f32 v128, v128, v22\n"
        "v_sub_f32 v132, v132, v23\n"
        "v_mul_f32 v112, 0.5, v112\n"
        "v_mul_f32 v116, 0.5, v116\n"
        "v_mul_f32 v128, 0.5, v128\n"
        "v_mul_f32 v132, 0.5, v132\n"
        "s_mov_b64 exec, -1\n"
        "s_cbranch_scc1 .Lgf_spq\n"
        ".Lgf_spq_ret:\n"
        "s_cmp_eq_u32 s60, 0\n"
        "s_waitcnt vmcnt(3)\n"
        "v_mov_b32 v176, v224\n"
        "v_mov_b32 v184, v226\n"
        "v_mov_b32 v180, v225\n"
        "v_mov_b32 v188, v227\n"
        "s_waitcnt vmcnt(2)\n"
        "v_mov_b32 v177, v228\n"
        "v_mov_b32 v185, v230\n"
        "v_mov_b32 v181, v229\n"
        "v_mov_b32 v189, v231\n"
        "s_waitcnt vmcnt(1)\n"
        "v_mov_b32 v178, v232\n"
        "v_mov_b32 v186, v234\n"
        "v_mov_b32 v182, v233\n"
        "v_mov_b32 v190, v235\n"
        "s_waitcnt vmcnt(0)\n"
        "v_mov_b32 v179, v236\n"
        "v_mov_b32 v187, v238\n"
        "v_mov_b32 v183, v237\n"
        "v_mov_b32 v191, v239\n"
        "s_setprio 1\n"
        "v_mfma_f32_16x16x32_bf16 v[192:195], v[24:27], v[176:179], 0\n"
        "v_mfma_f32_16x16x32_bf16 v[196:199], v[28:31], v[176:179], 0\n"
        "s_nop 1\n"
        "v_mfma_f32_16x16x32_bf16 v[208:211], v[24:27], v[184:187], 0\n"
        "s_nop 1\n"
        "v_mfma_f32_16x16x32_bf16 v[212:215], v[28:31], v[184:187], 0\n"
        "s_nop 1\n"
        "v_mfma_f32_16x16x32_bf16 v[200:203], v[24:27], v[180:183], 0\n"
        "v_cvt_pk_bf16_f32 v224, v192, v196\n"
        "v_cvt_pk_bf16_f32 v226, v193, v197\n"
        "v_mfma_f32_16x16x32_bf16 v[204:207], v[28:31], v[180:183], 0\n"
        "v_cvt_pk_bf16_f32 v228, v194, v198\n"
        "v_cvt_pk_bf16_f32 v230, v195, v199\n"
        "v_mfma_f32_16x16x32_bf16 v[216:219], v[24:27], v[188:191], 0\n"
        "v_cvt_pk_bf16_f32 v225, v208, v212\n"
        "v_cvt_pk_bf16_f32 v227, v209, v213\n"
        "v_mfma_f32_16x16x32_bf16 v[220:223], v[28:31], v[188:191], 0\n"
        "ds_write2st64_b64 v9, v[224:225], v[226:227] offset0:0 offset1:4\n"
        "v_cvt_pk_bf16_f32 v229, v210, v214\n"
        "v_cvt_pk_bf16_f32 v231, v211, v215\n"
        "ds_write2st64_b64 v9, v[228:229], v[230:231] offset0:8 offset1:12\n"
        "v_cvt_pk_bf16_f32 v224, v200, v204\n"
        "v_cvt_pk_bf16_f32 v226, v201, v205\n"
        "v_cvt_pk_bf16_f32 v228, v202, v206\n"
        "v_cvt_pk_bf16_f32 v230, v203, v207\n"
        "v_cvt_pk_bf16_f32 v225, v216, v220\n"
        "v_cvt_pk_bf16_f32 v227, v217, v221\n"
        "ds_write2st64_b64 v9, v[224:225], v[226:227] offset0:2 offset1:6\n"
        "v_cvt_pk_bf16_f32 v229, v218, v222\n"
        "v_cvt_pk_bf16_f32 v231, v219, v223\n"
        "ds_write2st64_b64 v9, v[228:229], v[230:231] offset0:10 offset1:14\n"
        "s_waitcnt lgkmcnt(0)\n"
        "s_barrier\n"
        "ds_read_b64 v[176:177], v8 offset:0\n"
        "ds_read_b64 v[178:179], v8 offset:512\n"
        "ds_read_b64 v[180:181], v8 offset:1024\n"
        "ds_read_b64 v[182:183], v8 offset:1536\n"
        "ds_read_b64 v[184:185], v8 offset:2048\n"
        "ds_read_b64 v[186:187], v8 offset:2560\n"
        "ds_read_b64 v[188:189], v8 offset:3072\n"
        "ds_read_b64 v[190:191], v8 offset:3584\n"
        "s_waitcnt lgkmcnt(6)\n"
        "v_mfma_f32_16x16x32_bf16 v[192:195], v[176:179], v[48:51], 0\n"
        "v_mfma_f32_16x16x32_bf16 v[196:199], v[176:179], v[52:55], 0\n"
        "s_waitcnt lgkmcnt(4)\n"
        "v_mfma_f32_16x16x32_bf16 v[200:203], v[180:183], v[48:51], 0\n"
        "v_mfma_f32_16x16x32_bf16 v[204:207], v[180:183], v[52:55], 0\n"
        "s_waitcnt lgkmcnt(2)\n"
        "v_mfma_f32_16x16x32_bf16 v[208:211], v[184:187], v[64:67], 0\n"
        "v_mfma_f32_16x16x32_bf16 v[212:215], v[184:187], v[68:71], 0\n"
        "s_waitcnt lgkmcnt(0)\n"
        "v_mfma_f32_16x16x32_bf16 v[216:219], v[188:191], v[64:67], 0\n"
        "v_mfma_f32_16x16x32_bf16 v[220:223], v[188:191], v[68:71], 0\n"
        "s_setprio 0\n"
        "v_fmac_f32 v192, v80, v196\n"
        "v_fmac_f32 v193, v81, v197\n"
        "v_fmac_f32 v194, v82, v198\n"
        "v_fmac_f32 v195, v83, v199\n"
        "v_fmac_f32 v196, v84, v192\n"
        "v_fmac_f32 v197, v85, v193\n"
        "v_fmac_f32 v198, v86, v194\n"
        "v_fmac_f32 v199, v87, v195\n"
        "v_fmac_f32 v192, v80, v196\n"
        "v_fmac_f32 v193, v81, v197\n"
        "v_fmac_f32 v194, v82, v198\n"
        "v_fmac_f32 v195, v83, v199\n"
        "v_fmac_f32 v200, v88, v204\n"
        "v_fmac_f32 v201, v89, v205\n"
        "v_fmac_f32 v202, v90, v206\n"
        "v_fmac_f32 v203, v91, v207\n"
        "v_fmac_f32 v204, v92, v200\n"
        "v_fmac_f32 v205, v93, v201\n"
        "v_fmac_f32 v206, v94, v202\n"
        "v_fmac_f32 v207, v95, v203\n"
        "v_fmac_f32 v200, v88, v204\n"
        "v_fmac_f32 v201, v89, v205\n"
        "v_fmac_f32 v202, v90, v206\n"
        "v_fmac_f32 v203, v91, v207\n"
        "v_fmac_f32 v208, v96, v212\n"
        "v_fmac_f32 v209, v97, v213\n"
        "v_fmac_f32 v210, v98, v214\n"
        "v_fmac_f32 v211, v99, v215\n"
        "v_fmac_f32 v212, v100, v208\n"
        "v_fmac_f32 v213, v101, v209\n"
        "v_fmac_f32 v214, v102, v210\n"
        "v_fmac_f32 v215, v103, v211\n"
        "v_fmac_f32 v208, v96, v212\n"
        "v_fmac_f32 v209, v97, v213\n"
        "v_fmac_f32 v210, v98, v214\n"
        "v_fmac_f32 v211, v99, v215\n"
        "v_fmac_f32 v216, v104, v220\n"
        "v_fmac_f32 v217, v105, v221\n"
        "v_fmac_f32 v218, v106, v222\n"
        "v_fmac_f32 v219, v107, v223\n"
        "v_fmac_f32 v220, v108, v216\n"
        "v_fmac_f32 v221, v109, v217\n"
        "v_fmac_f32 v222, v110, v218\n"
        "v_fmac_f32 v223, v111, v219\n"
        "v_fmac_f32 v216, v104, v220\n"
        "v_fmac_f32 v217, v105, v221\n"
        "v_fmac_f32 v218, v106, v222\n"
        "v_fmac_f32 v219, v107, v223\n"
        "s_setprio 1\n"
        "v_cvt_pk_bf16_f32 v176, v192, v196\n"
        "v_cvt_pk_bf16_f32 v177, v193, v197\n"
        "v_cvt_pk_bf16_f32 v178, v194, v198\n"
        "v_cvt_pk_bf16_f32 v179, v195, v199\n"
        "v_cvt_pk_bf16_f32 v184, v208, v212\n"
        "v_cvt_pk_bf16_f32 v185, v209, v213\n"
        "v_mfma_f32_16x16x32_bf16 v[232:235], v[48:51], v[176:179], 0\n"
        "v_cvt_pk_bf16_f32 v186, v210, v214\n"
        "v_cvt_pk_bf16_f32 v187, v211, v215\n"
        "v_mfma_f32_16x16x32_bf16 v[236:239], v[52:55], v[176:179], 0\n"
        "v_cvt_pk_bf16_f32 v180, v200, v204\n"
        "v_cvt_pk_bf16_f32 v181, v201, v205\n"
        "v_mfma_f32_16x16x32_bf16 v[248:251], v[48:51], v[184:187], 0\n"
        "v_cvt_pk_bf16_f32 v182, v202, v206\n"
        "v_cvt_pk_bf16_f32 v183, v203, v207\n"
        "v_mfma_f32_16x16x32_bf16 v[252:255], v[52:55], v[184:187], 0\n"
        "v_cvt_pk_bf16_f32 v188, v216, v220\n"
        "v_cvt_pk_bf16_f32 v189, v217, v221\n"
        "v_mfma_f32_16x16x32_bf16 v[224:227], v[56:59], v[180:183], v[232:235]\n"
        "v_cvt_pk_bf16_f32 v190, v218, v222\n"
        "v_cvt_pk_bf16_f32 v191, v219, v223\n"
        "v_mfma_f32_16x16x32_bf16 v[228:231], v[60:63], v[180:183], v[236:239]\n"
        "s_nop 0\n"
        "v_mfma_f32_16x16x32_bf16 v[240:243], v[56:59], v[188:191], v[248:251]\n"
        "v_mfma_f32_16x16x32_bf16 v[244:247], v[60:63], v[188:191], v[252:255]\n"
        "s_setprio 0\n"
        "s_nop 0\n"
        "v_fma_f32 v232, v232, 2.0, -v224\n"
        "v_fma_f32 v233, v233, 2.0, -v225\n"
        "v_fma_f32 v234, v234, 2.0, -v226\n"
        "v_fma_f32 v235, v235, 2.0, -v227\n"
        "v_fma_f32 v236, v236, 2.0, -v228\n"
        "v_fma_f32 v237, v237, 2.0, -v229\n"
        "v_fma_f32 v238, v238, 2.0, -v230\n"
        "v_fma_f32 v239, v239, 2.0, -v231\n"
        "v_fma_f32 v248, v248, 2.0, -v240\n"
        "v_fma_f32 v249, v249, 2.0, -v241\n"
        "v_fma_f32 v250, v250, 2.0, -v242\n"
        "v_fma_f32 v251, v251, 2.0, -v243\n"
        "v_fma_f32 v252, v252, 2.0, -v244\n"
        "v_fma_f32 v253, v253, 2.0, -v245\n"
        "v_fma_f32 v254, v254, 2.0, -v246\n"
        "v_fma_f32 v255, v255, 2.0, -v247\n"
        "s_cbranch_scc1 .Lgf_spz\n"
        ".Lgf_spz_ret:\n"
        "s_setprio 0\n"
        "v_mul_f32 v200, v224, v112\n"
        "v_mul_f32 v201, v225, v113\n"
        "v_mul_f32 v202, v226, v114\n"
        "v_mul_f32 v203, v227, v115\n"
        "v_mul_f32 v204, v224, v116\n"
        "v_mul_f32 v205, v225, v117\n"
        "v_mul_f32 v206, v226, v118\n"
        "v_mul_f32 v207, v227, v119\n"
        "v_fma_f32 v200, -v228, v116, v200\n"
        "v_fma_f32 v201, -v229, v117, v201\n"
        "v_fma_f32 v202, -v230, v118, v202\n"
        "v_fma_f32 v203, -v231, v119, v203\n"
        "v_fmac_f32 v204, v228, v112\n"
        "v_fmac_f32 v205, v229, v113\n"
        "v_fmac_f32 v206, v230, v114\n"
        "v_fmac_f32 v207, v231, v115\n"
        "v_mul_f32 v216, v240, v120\n"
        "v_mul_f32 v217, v241, v121\n"
        "v_mul_f32 v218, v242, v122\n"
        "v_mul_f32 v219, v243, v123\n"
        "v_mul_f32 v220, v240, v124\n"
        "v_mul_f32 v221, v241, v125\n"
        "v_mul_f32 v222, v242, v126\n"
        "v_mul_f32 v223, v243, v127\n"
        "v_fma_f32 v216, -v244, v124, v216\n"
        "v_fma_f32 v217, -v245, v125, v217\n"
        "v_fma_f32 v218, -v246, v126, v218\n"
        "v_fma_f32 v219, -v247, v127, v219\n"
        "v_fmac_f32 v220, v244, v120\n"
        "v_fmac_f32 v221, v245, v121\n"
        "v_fmac_f32 v222, v246, v122\n"
        "v_fmac_f32 v223, v247, v123\n"
        "v_fma_f32 v208, v248, v136, v216\n"
        "v_fma_f32 v209, v249, v137, v217\n"
        "v_fma_f32 v210, v250, v138, v218\n"
        "v_fma_f32 v211, v251, v139, v219\n"
        "v_fma_f32 v212, -v248, v140, v220\n"
        "v_fma_f32 v213, -v249, v141, v221\n"
        "v_fma_f32 v214, -v250, v142, v222\n"
        "v_fma_f32 v215, -v251, v143, v223\n"
        "v_fmac_f32 v208, v252, v140\n"
        "v_fmac_f32 v209, v253, v141\n"
        "v_fmac_f32 v210, v254, v142\n"
        "v_fmac_f32 v211, v255, v143\n"
        "v_fmac_f32 v212, v252, v136\n"
        "v_fmac_f32 v213, v253, v137\n"
        "v_fmac_f32 v214, v254, v138\n"
        "v_fmac_f32 v215, v255, v139\n"
        "v_fma_f32 v192, v232, v128, v200\n"
        "v_fma_f32 v193, v233, v129, v201\n"
        "v_fma_f32 v194, v234, v130, v202\n"
        "v_fma_f32 v195, v235, v131, v203\n"
        "v_fma_f32 v196, -v232, v132, v204\n"
        "v_fma_f32 v197, -v233, v133, v205\n"
        "v_fma_f32 v198, -v234, v134, v206\n"
        "v_fma_f32 v199, -v235, v135, v207\n"
        "v_fmac_f32 v192, v236, v132\n"
        "v_fmac_f32 v193, v237, v133\n"
        "v_fmac_f32 v194, v238, v134\n"
        "v_fmac_f32 v195, v239, v135\n"
        "v_fmac_f32 v196, v236, v128\n"
        "v_fmac_f32 v197, v237, v129\n"
        "v_fmac_f32 v198, v238, v130\n"
        "v_fmac_f32 v199, v239, v131\n"
        "s_cbranch_scc1 .Lgf_spm\n"
        ".Lgf_spm_ret:\n"
        "v_fma_f32 v216, v216, 2.0, -v208\n"
        "v_fma_f32 v217, v217, 2.0, -v209\n"
        "v_fma_f32 v218, v218, 2.0, -v210\n"
        "v_fma_f32 v219, v219, 2.0, -v211\n"
        "v_fma_f32 v220, v220, 2.0, -v212\n"
        "v_fma_f32 v221, v221, 2.0, -v213\n"
        "v_fma_f32 v222, v222, 2.0, -v214\n"
        "v_fma_f32 v223, v223, 2.0, -v215\n"
        "v_fma_f32 v200, v200, 2.0, -v192\n"
        "v_fma_f32 v201, v201, 2.0, -v193\n"
        "v_fma_f32 v202, v202, 2.0, -v194\n"
        "v_fma_f32 v203, v203, 2.0, -v195\n"
        "v_fma_f32 v204, v204, 2.0, -v196\n"
        "v_fma_f32 v205, v205, 2.0, -v197\n"
        "v_fma_f32 v206, v206, 2.0, -v198\n"
        "v_fma_f32 v207, v207, 2.0, -v199\n"
        "s_setprio 1\n"
        "v_cvt_pk_bf16_f32 v176, v196, v192\n"
        "v_cvt_pk_bf16_f32 v177, v197, v193\n"
        "v_cvt_pk_bf16_f32 v178, v198, v194\n"
        "v_cvt_pk_bf16_f32 v179, v199, v195\n"
        "v_cvt_pk_bf16_f32 v184, v212, v208\n"
        "v_cvt_pk_bf16_f32 v185, v213, v209\n"
        "v_mfma_f32_16x16x32_bf16 v[224:227], v[176:179], v[48:51], 0\n"
        "v_cvt_pk_bf16_f32 v186, v214, v210\n"
        "v_cvt_pk_bf16_f32 v187, v215, v211\n"
        "v_mfma_f32_16x16x32_bf16 v[228:231], v[176:179], v[52:55], 0\n"
        "v_cvt_pk_bf16_f32 v180, v204, v200\n"
        "v_cvt_pk_bf16_f32 v181, v205, v201\n"
        "v_mfma_f32_16x16x32_bf16 v[240:243], v[184:187], v[48:51], 0\n"
        "v_cvt_pk_bf16_f32 v182, v206, v202\n"
        "v_cvt_pk_bf16_f32 v183, v207, v203\n"
        "v_mfma_f32_16x16x32_bf16 v[244:247], v[184:187], v[52:55], 0\n"
        "v_cvt_pk_bf16_f32 v188, v220, v216\n"
        "v_cvt_pk_bf16_f32 v189, v221, v217\n"
        "v_mfma_f32_16x16x32_bf16 v[232:235], v[180:183], v[64:67], 0\n"
        "v_cvt_pk_bf16_f32 v190, v222, v218\n"
        "v_cvt_pk_bf16_f32 v191, v223, v219\n"
        "v_mfma_f32_16x16x32_bf16 v[236:239], v[180:183], v[68:71], 0\n"
        "s_nop 0\n"
        "v_mfma_f32_16x16x32_bf16 v[248:251], v[188:191], v[64:67], 0\n"
        "v_mfma_f32_16x16x32_bf16 v[252:255], v[188:191], v[68:71], 0\n"
        "s_setprio 0\n"
        "v_fmac_f32 v224, v80, v228\n"
        "v_fmac_f32 v225, v81, v229\n"
        "v_fmac_f32 v226, v82, v230\n"
        "v_fmac_f32 v227, v83, v231\n"
        "v_fmac_f32 v228, v84, v224\n"
        "v_fmac_f32 v229, v85, v225\n"
        "v_fmac_f32 v230, v86, v226\n"
        "v_fmac_f32 v231, v87, v227\n"
        "v_fmac_f32 v224, v80, v228\n"
        "v_fmac_f32 v225, v81, v229\n"
        "v_fmac_f32 v226, v82, v230\n"
        "v_fmac_f32 v227, v83, v231\n"
        "v_fmac_f32 v240, v88, v244\n"
        "v_fmac_f32 v241, v89, v245\n"
        "v_fmac_f32 v242, v90, v246\n"
        "v_fmac_f32 v243, v91, v247\n"
        "v_fmac_f32 v244, v92, v240\n"
        "v_fmac_f32 v245, v93, v241\n"
        "v_fmac_f32 v246, v94, v242\n"
        "v_fmac_f32 v247, v95, v243\n"
        "v_fmac_f32 v240, v88, v244\n"
        "v_fmac_f32 v241, v89, v245\n"
        "v_fmac_f32 v242, v90, v246\n"
        "v_fmac_f32 v243, v91, v247\n"
        "v_fmac_f32 v232, v96, v236\n"
        "v_fmac_f32 v233, v97, v237\n"
        "v_fmac_f32 v234, v98, v238\n"
        "v_fmac_f32 v235, v99, v239\n"
        "v_fmac_f32 v236, v100, v232\n"
        "v_fmac_f32 v237, v101, v233\n"
        "v_fmac_f32 v238, v102, v234\n"
        "v_fmac_f32 v239, v103, v235\n"
        "v_fmac_f32 v232, v96, v236\n"
        "v_fmac_f32 v233, v97, v237\n"
        "v_fmac_f32 v234, v98, v238\n"
        "v_fmac_f32 v235, v99, v239\n"
        "v_fmac_f32 v248, v104, v252\n"
        "v_fmac_f32 v249, v105, v253\n"
        "v_fmac_f32 v250, v106, v254\n"
        "v_fmac_f32 v251, v107, v255\n"
        "v_fmac_f32 v252, v108, v248\n"
        "v_fmac_f32 v253, v109, v249\n"
        "v_fmac_f32 v254, v110, v250\n"
        "v_fmac_f32 v255, v111, v251\n"
        "v_fmac_f32 v248, v104, v252\n"
        "v_fmac_f32 v249, v105, v253\n"
        "v_fmac_f32 v250, v106, v254\n"
        "v_fmac_f32 v251, v107, v255\n"
        "s_setprio 1\n"
        "v_cvt_pk_bf16_f32 v176, v224, v228\n"
        "v_cvt_pk_bf16_f32 v177, v225, v229\n"
        "v_cvt_pk_bf16_f32 v178, v226, v230\n"
        "v_cvt_pk_bf16_f32 v179, v227, v231\n"
        "v_cvt_pk_bf16_f32 v184, v240, v244\n"
        "v_cvt_pk_bf16_f32 v185, v241, v245\n"
        "v_mfma_f32_16x16x32_bf16 v[192:195], v[48:51], v[176:179], 0\n"
        "v_cvt_pk_bf16_f32 v186, v242, v246\n"
        "v_cvt_pk_bf16_f32 v187, v243, v247\n"
        "v_mfma_f32_16x16x32_bf16 v[196:199], v[52:55], v[176:179], 0\n"
        "v_cvt_pk_bf16_f32 v180, v232, v236\n"
        "v_cvt_pk_bf16_f32 v181, v233, v237\n"
        "v_mfma_f32_16x16x32_bf16 v[208:211], v[56:59], v[184:187], 0\n"
        "v_cvt_pk_bf16_f32 v182, v234, v238\n"
        "v_cvt_pk_bf16_f32 v183, v235, v239\n"
        "v_mfma_f32_16x16x32_bf16 v[212:215], v[60:63], v[184:187], 0\n"
        "v_cvt_pk_bf16_f32 v188, v248, v252\n"
        "v_cvt_pk_bf16_f32 v189, v249, v253\n"
        "v_mfma_f32_16x16x32_bf16 v[200:203], v[48:51], v[180:183], 0\n"
        "v_cvt_pk_bf16_f32 v190, v250, v254\n"
        "v_cvt_pk_bf16_f32 v191, v251, v255\n"
        "v_mfma_f32_16x16x32_bf16 v[204:207], v[52:55], v[180:183], 0\n"
        "v_cvt_pk_bf16_f32 v224, v196, v192\n"
        "v_cvt_pk_bf16_f32 v226, v197, v193\n"
        "v_mfma_f32_16x16x32_bf16 v[216:219], v[56:59], v[188:191], 0\n"
        "v_cvt_pk_bf16_f32 v225, v212, v208\n"
        "v_cvt_pk_bf16_f32 v227, v213, v209\n"
        "v_mfma_f32_16x16x32_bf16 v[220:223], v[60:63], v[188:191], 0\n"
        "ds_write2st64_b64 v9, v[224:225], v[226:227] offset0:64 offset1:68\n"
        "v_cvt_pk_bf16_f32 v228, v198, v194\n"
        "v_cvt_pk_bf16_f32 v230, v199, v195\n"
        "v_cvt_pk_bf16_f32 v229, v214, v210\n"
        "v_cvt_pk_bf16_f32 v231, v215, v211\n"
        "ds_write2st64_b64 v9, v[228:229], v[230:231] offset0:72 offset1:76\n"
        "v_cvt_pk_bf16_f32 v224, v204, v200\n"
        "v_cvt_pk_bf16_f32 v226, v205, v201\n"
        "v_cvt_pk_bf16_f32 v225, v220, v216\n"
        "v_cvt_pk_bf16_f32 v227, v221, v217\n"
        "ds_write2st64_b64 v9, v[224:225], v[226:227] offset0:66 offset1:70\n"
        "v_cvt_pk_bf16_f32 v228, v206, v202\n"
        "v_cvt_pk_bf16_f32 v230, v207, v203\n"
        "v_cvt_pk_bf16_f32 v229, v222, v218\n"
        "v_cvt_pk_bf16_f32 v231, v223, v219\n"
        "ds_write2st64_b64 v9, v[228:229], v[230:231] offset0:74 offset1:78\n"
        "s_setprio 0\n"
        "s_waitcnt lgkmcnt(0)\n"
        "s_barrier\n"
        "ds_read_b64 v[176:177], v8 offset:32768\n"
        "ds_read_b64 v[178:179], v8 offset:33280\n"
        "ds_read_b64 v[180:181], v8 offset:33792\n"
        "ds_read_b64 v[182:183], v8 offset:34304\n"
        "ds_read_b64 v[184:185], v8 offset:34816\n"
        "ds_read_b64 v[186:187], v8 offset:35328\n"
        "ds_read_b64 v[188:189], v8 offset:35840\n"
        "ds_read_b64 v[190:191], v8 offset:36352\n"
        "s_waitcnt lgkmcnt(6)\n"
        "v_mfma_f32_16x16x32_bf16 v[192:195], v[32:35], v[176:179], 0\n"
        "s_waitcnt lgkmcnt(4)\n"
        "v_mfma_f32_16x16x32_bf16 v[196:199], v[32:35], v[180:183], 0\n"
        "s_waitcnt lgkmcnt(2)\n"
        "v_mfma_f32_16x16x32_bf16 v[200:203], v[32:35], v[184:187], 0\n"
        "s_waitcnt lgkmcnt(0)\n"
        "v_mfma_f32_16x16x32_bf16 v[204:207], v[32:35], v[188:191], 0\n"
        "v_mfma_f32_16x16x32_bf16 v[208:211], v[36:39], v[176:179], 0\n"
        "s_nop 0\n"
        "v_cvt_pk_bf16_f32 v224, v192, v193\n"
        "v_mfma_f32_16x16x32_bf16 v[212:215], v[36:39], v[180:183], 0\n"
        "v_cvt_pk_bf16_f32 v225, v196, v197\n"
        "v_cvt_pk_bf16_f32 v228, v194, v195\n"
        "v_mfma_f32_16x16x32_bf16 v[216:219], v[36:39], v[184:187], 0\n"
        "v_cvt_pk_bf16_f32 v226, v200, v201\n"
        "v_cvt_pk_bf16_f32 v227, v204, v205\n"
        "buffer_store_dwordx4 v[224:227], v3, s[12:15], 0 offen offset:0 sc1\n"
        "v_mfma_f32_16x16x32_bf16 v[220:223], v[36:39], v[188:191], 0\n"
        "v_cvt_pk_bf16_f32 v229, v198, v199\n"
        "v_cvt_pk_bf16_f32 v230, v202, v203\n"
        "v_cvt_pk_bf16_f32 v231, v206, v207\n"
        "buffer_store_dwordx4 v[228:231], v3, s[12:15], 0 offen offset:256 sc1\n"
        "v_cvt_pk_bf16_f32 v232, v208, v209\n"
        "v_cvt_pk_bf16_f32 v233, v212, v213\n"
        "v_cvt_pk_bf16_f32 v234, v216, v217\n"
        "v_cvt_pk_bf16_f32 v236, v210, v211\n"
        "v_cvt_pk_bf16_f32 v235, v220, v221\n"
        "buffer_store_dwordx4 v[232:235], v3, s[12:15], 0 offen offset:512 sc1\n"
        "v_cvt_pk_bf16_f32 v237, v214, v215\n"
        "v_cvt_pk_bf16_f32 v238, v218, v219\n"
        "v_cvt_pk_bf16_f32 v239, v222, v223\n"
        "buffer_store_dwordx4 v[236:239], v3, s[12:15], 0 offen offset:768 sc1\n"
        "ds_read_b128 v[24:27], v1 offset:22528\n"
        "ds_read_b128 v[28:31], v1 offset:23552\n"
        "v_bfe_u32 v112, v1, 4, 4\n"
        "v_lshrrev_b32 v113, 8, v1\n"
        "s_lshl_b32 s34, s35, 1\n"
        "s_add_u32 s34, s34, 33792\n"
        "s_and_b32 s74, s74, 1\n"
        "v_xor_b32 v144, s74, v112\n"
        "v_lshl_add_u32 v10, v113, 12, s34\n"
        "v_lshl_add_u32 v10, v144, 3, v10\n"
        "v_and_b32 v144, 1, v113\n"
        "v_add_u32 v11, s31, v1\n"
        "v_lshl_add_u32 v11, v144, 3, v11\n"
        "v_xor_b32 v12, 8, v11\n"
        "s_lshl_b32 s34, s22, 14\n"
        "s_add_u32 s34, s34, s27\n"
        "v_add_u32 v13, s34, v13\n"
        "v_add_u32 v17, 16384, v10\n"
        "v_add_u32 v18, 2048, v10\n"
        "v_add_u32 v19, 18432, v10\n"
        "s_mov_b32 s43, 0x400000\n"
        "s_mov_b32 s44, 0x440000\n"
        "s_mov_b32 s45, 0x480000\n"
        "s_mov_b32 s46, 0x4c0000\n"
        "s_cmp_eq_u32 s22, 0\n"
        "s_cselect_b32 s66, -1, 0\n"
        "s_mov_b32 s67, 0\n"
        "s_mov_b32 s62, 0x33333333\n"
        "s_mov_b32 s64, 0x33333333\n"
        "s_cmov_b32 s62, 0x11111111\n"
        "s_cmov_b32 s64, 0x22222222\n"
        "s_mov_b32 s63, 0xcccccccc\n"
        "s_mov_b32 s65, 0xcccccccc\n"
        "s_lshl_b32 s34, s22, 4\n"
        "v_mov_b32 v145, 0x43800000\n"
        "v_mov_b32 v144, 0x43000000\n"
        "v_cndmask_b32_e64 v145, v145, v144, s[66:67]\n"
        "v_lshrrev_b32 v144, 1, v113\n"
        "v_lshl_add_u32 v144, v144, 3, s34\n"
        "v_cvt_f32_u32 v144, v144\n"
        "v_and_b32 v146, 1, v113\n"
        "v_cvt_f32_u32 v146, v146\n"
        "v_mul_f32 v146, 4.0, v146\n"
        "v_fmac_f32 v144, v146, v145\n"
        "v_and_b32 v146, 1, v112\n"
        "v_cvt_f32_u32 v146, v146\n"
        "v_mul_f32 v146, 0xbe800000, v146\n"
        "v_cndmask_b32_e64 v146, v146, 0, s[66:67]\n"
        "v_lshrrev_b32 v148, 2, v112\n"
        "v_cvt_f32_u32 v148, v148\n"
        "s_and_b32 s66, s66, 0xffff\n"
        "v_mul_f32 v150, 0.5, v148\n"
        "v_mov_b32 v147, 0x3e800000\n"
        "v_cndmask_b32_e64 v147, v147, v150, s[66:67]\n"
        "v_add_f32 v149, 4.0, v148\n"
        "v_mul_f32 v148, 0x3a000000, v148\n"
        "v_mul_f32 v149, 0x3a000000, v149\n"
        "v_fma_f32 v152, v148, v144, v146\n"
        "v_fma_f32 v156, v149, v144, v146\n"
        "v_fma_f32 v153, v148, v145, v152\n"
        "v_fma_f32 v157, v149, v145, v156\n"
        "v_fma_f32 v154, v148, v145, v153\n"
        "v_fma_f32 v158, v149, v145, v157\n"
        "v_fma_f32 v155, v148, v145, v154\n"
        "v_fma_f32 v159, v149, v145, v158\n"
        "s_waitcnt vmcnt(0)\n"
        "s_mov_b64 exec, 0xf\n"
        "buffer_store_dwordx2 v[6:7], v4, s[12:15], 0 offen offset:0 sc1\n"
        "s_mov_b64 exec, -1\n"
        ".Lgf_pub1:\n"
        "v_cos_f32 v160, v152\n"
        "v_add_f32 v152, v147, v152\n"
        "v_cos_f32 v152, v152\n"
        "v_cos_f32 v161, v153\n"
        "v_sin_f32 v153, v153\n"
        "v_cos_f32 v162, v154\n"
        "v_sin_f32 v154, v154\n"
        "v_cos_f32 v163, v155\n"
        "v_sin_f32 v155, v155\n"
        "v_cos_f32 v164, v156\n"
        "v_add_f32 v156, v147, v156\n"
        "v_cos_f32 v156, v156\n"
        "v_cos_f32 v165, v157\n"
        "v_sin_f32 v157, v157\n"
        "v_cos_f32 v166, v158\n"
        "v_sin_f32 v158, v158\n"
        "v_cos_f32 v167, v159\n"
        "v_sin_f32 v159, v159\n"
        "v_cvt_pk_bf16_f32 v160, v160, v152\n"
        "v_cvt_pk_bf16_f32 v161, v161, -v153\n"
        "v_cvt_pk_bf16_f32 v162, v162, -v154\n"
        "v_cvt_pk_bf16_f32 v163, v163, -v155\n"
        "v_cvt_pk_bf16_f32 v164, v164, v156\n"
        "v_cvt_pk_bf16_f32 v165, v165, -v157\n"
        "v_cvt_pk_bf16_f32 v166, v166, -v158\n"
        "v_cvt_pk_bf16_f32 v167, v167, -v159\n"
        "v_cndmask_b32_e64 v40, 0, v160, s[62:63]\n"
        "v_cndmask_b32_e64 v41, 0, v161, s[64:65]\n"
        "v_cndmask_b32_e64 v42, 0, v162, s[62:63]\n"
        "v_cndmask_b32_e64 v43, 0, v163, s[64:65]\n"
        "v_cndmask_b32_e64 v44, 0, v164, s[62:63]\n"
        "v_cndmask_b32_e64 v45, 0, v165, s[64:65]\n"
        "v_cndmask_b32_e64 v46, 0, v166, s[62:63]\n"
        "v_cndmask_b32_e64 v47, 0, v167, s[64:65]\n"
        "s_mov_b32 s57, 0x20000\n"
        "s_mov_b64 exec, 0xffff\n"
        ".Lgf_poll1:\n"
        "v_mov_b32 v176, 0\n"
        "v_mov_b32 v177, 0\n"
        "buffer_atomic_or_x2 v[176:177], v5, s[12:15], 0 offen offset:0 sc0 sc1\n"
        "s_sub_u32 s57, s57, 1\n"
        "s_cbranch_scc1 .Lgf_go1\n"
        "s_waitcnt vmcnt(0)\n"
        "v_cmp_ne_u64 vcc, v[176:177], v[6:7]\n"
        "s_cbranch_vccnz .Lgf_poll1\n"
        ".Lgf_go1:\n"
        "s_mov_b64 exec, -1\n"
        "buffer_load_dwordx4 v[224:227], v2, s[12:15], 0 offen sc1\n"
        "buffer_load_dwordx4 v[228:231], v2, s[12:15], s47 offen sc1\n"
        "buffer_load_dwordx4 v[232:235], v2, s[12:15], s48 offen sc1\n"
        "buffer_load_dwordx4 v[236:239], v2, s[12:15], s49 offen sc1\n"
        "s_waitcnt vmcnt(3)\n"
        "v_mov_b32 v176, v224\n"
        "v_mov_b32 v184, v226\n"
        "v_mov_b32 v180, v225\n"
        "v_mov_b32 v188, v227\n"
        "s_waitcnt vmcnt(2)\n"
        "v_mov_b32 v177, v228\n"
        "v_mov_b32 v185, v230\n"
        "v_mov_b32 v181, v229\n"
        "v_mov_b32 v189, v231\n"
        "s_waitcnt vmcnt(1)\n"
        "v_mov_b32 v178, v232\n"
        "v_mov_b32 v186, v234\n"
        "v_mov_b32 v182, v233\n"
        "v_mov_b32 v190, v235\n"
        "s_waitcnt vmcnt(0)\n"
        "v_mov_b32 v179, v236\n"
        "v_mov_b32 v187, v238\n"
        "v_mov_b32 v183, v237\n"
        "v_mov_b32 v191, v239\n"
        "v_mfma_f32_16x16x32_bf16 v[192:195], v[40:43], v[176:179], 0\n"
        "s_nop 1\n"
        "v_mfma_f32_16x16x32_bf16 v[200:203], v[40:43], v[184:187], 0\n"
        "s_nop 1\n"
        "v_mfma_f32_16x16x32_bf16 v[208:211], v[44:47], v[176:179], 0\n"
        "s_nop 1\n"
        "v_mfma_f32_16x16x32_bf16 v[216:219], v[44:47], v[184:187], 0\n"
        "v_cvt_pk_bf16_f32 v240, v192, v193\n"
        "v_cvt_pk_bf16_f32 v241, v194, v195\n"
        "v_mfma_f32_16x16x32_bf16 v[196:199], v[40:43], v[180:183], 0\n"
        "v_cvt_pk_bf16_f32 v242, v200, v201\n"
        "v_cvt_pk_bf16_f32 v243, v202, v203\n"
        "v_mfma_f32_16x16x32_bf16 v[204:207], v[40:43], v[188:191], 0\n"
        "v_cvt_pk_bf16_f32 v244, v208, v209\n"
        "v_cvt_pk_bf16_f32 v245, v210, v211\n"
        "ds_write2_b64 v10, v[240:241], v[242:243] offset0:0 offset1:16\n"
        "v_mfma_f32_16x16x32_bf16 v[212:215], v[44:47], v[180:183], 0\n"
        "v_cvt_pk_bf16_f32 v246, v216, v217\n"
        "v_cvt_pk_bf16_f32 v247, v218, v219\n"
        "v_mfma_f32_16x16x32_bf16 v[220:223], v[44:47], v[188:191], 0\n"
        "v_cvt_pk_bf16_f32 v248, v196, v197\n"
        "v_cvt_pk_bf16_f32 v249, v198, v199\n"
        "ds_write2_b64 v17, v[244:245], v[246:247] offset0:0 offset1:16\n"
        "v_cvt_pk_bf16_f32 v250, v204, v205\n"
        "v_cvt_pk_bf16_f32 v251, v206, v207\n"
        "ds_write2_b64 v18, v[248:249], v[250:251] offset0:0 offset1:16\n"
        "v_cvt_pk_bf16_f32 v252, v212, v213\n"
        "v_cvt_pk_bf16_f32 v253, v214, v215\n"
        "v_cvt_pk_bf16_f32 v254, v220, v221\n"
        "v_cvt_pk_bf16_f32 v255, v222, v223\n"
        "ds_write2_b64 v19, v[252:253], v[254:255] offset0:0 offset1:16\n"
        "s_waitcnt lgkmcnt(0)\n"
        "s_barrier\n"
        "ds_read_b64 v[176:177], v11 offset:0\n"
        "ds_read_b64 v[178:179], v11 offset:1024\n"
        "ds_read_b64 v[180:181], v11 offset:2048\n"
        "ds_read_b64 v[182:183], v11 offset:3072\n"
        "ds_read_b64 v[184:185], v12 offset:0\n"
        "ds_read_b64 v[186:187], v12 offset:1024\n"
        "ds_read_b64 v[188:189], v12 offset:2048\n"
        "ds_read_b64 v[190:191], v12 offset:3072\n"
        "s_waitcnt lgkmcnt(6)\n"
        "v_mfma_f32_16x16x32_bf16 v[192:195], v[24:27], v[176:179], 0\n"
        "s_waitcnt lgkmcnt(4)\n"
        "v_mfma_f32_16x16x32_bf16 v[196:199], v[24:27], v[180:183], 0\n"
        "s_waitcnt lgkmcnt(2)\n"
        "v_mfma_f32_16x16x32_bf16 v[200:203], v[24:27], v[184:187], 0\n"
        "s_waitcnt lgkmcnt(0)\n"
        "v_mfma_f32_16x16x32_bf16 v[204:207], v[24:27], v[188:191], 0\n"
        "v_mfma_f32_16x16x32_bf16 v[208:211], v[28:31], v[176:179], 0\n"
        "s_nop 1\n"
        "v_mfma_f32_16x16x32_bf16 v[212:215], v[28:31], v[180:183], 0\n"
        "v_cvt_pk_bf16_f32 v224, v192, v196\n"
        "v_cvt_pk_bf16_f32 v226, v193, v197\n"
        "v_mfma_f32_16x16x32_bf16 v[216:219], v[28:31], v[184:187], 0\n"
        "v_cvt_pk_bf16_f32 v228, v194, v198\n"
        "v_cvt_pk_bf16_f32 v230, v195, v199\n"
        "v_mfma_f32_16x16x32_bf16 v[220:223], v[28:31], v[188:191], 0\n"
        "v_cvt_pk_bf16_f32 v225, v200, v204\n"
        "buffer_store_dwordx2 v[224:225], v13, s[8:11], 0 offen sc1 nt\n"
        "v_cvt_pk_bf16_f32 v227, v201, v205\n"
        "buffer_store_dwordx2 v[226:227], v13, s[8:11], s37 offen sc1 nt\n"
        "v_cvt_pk_bf16_f32 v229, v202, v206\n"
        "buffer_store_dwordx2 v[228:229], v13, s[8:11], s39 offen sc1 nt\n"
        "v_cvt_pk_bf16_f32 v231, v203, v207\n"
        "buffer_store_dwordx2 v[230:231], v13, s[8:11], s41 offen sc1 nt\n"
        "v_cvt_pk_bf16_f32 v232, v208, v212\n"
        "v_cvt_pk_bf16_f32 v233, v216, v220\n"
        "buffer_store_dwordx2 v[232:233], v13, s[8:11], s43 offen sc1 nt\n"
        "v_cvt_pk_bf16_f32 v234, v209, v213\n"
        "v_cvt_pk_bf16_f32 v235, v217, v221\n"
        "buffer_store_dwordx2 v[234:235], v13, s[8:11], s44 offen sc1 nt\n"
        "v_cvt_pk_bf16_f32 v236, v210, v214\n"
        "v_cvt_pk_bf16_f32 v237, v218, v222\n"
        "buffer_store_dwordx2 v[236:237], v13, s[8:11], s45 offen sc1 nt\n"
        "v_cvt_pk_bf16_f32 v238, v211, v215\n"
        "v_cvt_pk_bf16_f32 v239, v219, v223\n"
        "buffer_store_dwordx2 v[238:239], v13, s[8:11], s46 offen sc1 nt\n"
        "s_cmp_eq_u32 s60, 0\n"
        "s_cbranch_scc1 .Lgf_ep\n"
        ".Lgf_ep_ret:\n"
        "s_endpgm\n"
        ".Lgf_aa:\n"
        "s_nop 7\n"
        "v_cndmask_b32_e64 v154, -v151, v151, vcc\n"
        "v_lshlrev_b32 v155, 31, v148\n"
        "v_xor_b32 v155, v155, v150\n"
        "v_cmp_eq_u32 s[62:63], 0, v147\n"
        "s_nop 1\n"
        "v_cndmask_b32_e64 v154, v154, v155, s[62:63]\n"
        "s_and_b64 s[62:63], s[62:63], vcc\n"
        "v_mov_b32 v156, 0x3e800000\n"
        "v_cndmask_b32_e64 v156, 1.0, v156, s[62:63]\n"
        "v_mul_f32 v150, v156, v150\n"
        "v_mul_f32 v154, v156, v154\n"
        "v_cvt_pk_bf16_f32 v154, v150, v154\n"
        "v_and_b32 v155, 1, v147\n"
        "v_cmp_eq_u32 s[62:63], 1, v155\n"
        "v_lshlrev_b32 v155, 4, v155\n"
        "v_and_b32 v152, 0xffff, v154\n"
        "v_lshrrev_b32 v153, 16, v154\n"
        "v_lshlrev_b32 v152, v155, v152\n"
        "v_lshlrev_b32 v153, v155, v153\n"
        "v_cndmask_b32_e64 v155, v154, 0, s[62:63]\n"
        "v_cndmask_b32_e64 v156, 0, v154, s[62:63]\n"
        "v_cndmask_b32 v152, v152, v155, vcc\n"
        "v_cndmask_b32 v153, v153, v156, vcc\n"
        "s_nop 7\n"
        "s_branch .Lgf_aa_ret\n"
        ".Lgf_a2:\n"
        "s_nop 7\n"
        "v_cmp_eq_u32 vcc, 0, v147\n"
        "v_and_b32 v160, 1, v147\n"
        "v_lshlrev_b32 v160, 4, v160\n"
        "v_cndmask_b32_e64 v161, -v154, v152, vcc\n"
        "v_cvt_pk_bf16_f32 v161, v152, v161\n"
        "v_and_b32 v162, 0xffff, v161\n"
        "v_lshrrev_b32 v161, 16, v161\n"
        "v_lshlrev_b32 v162, v160, v162\n"
        "v_lshlrev_b32 v161, v160, v161\n"
        "v_cndmask_b32_e64 v156, v156, v162, s[62:63]\n"
        "v_cndmask_b32_e64 v157, v157, v161, s[62:63]\n"
        "v_cndmask_b32_e64 v161, -v155, -v153, vcc\n"
        "v_cvt_pk_bf16_f32 v161, v153, v161\n"
        "v_and_b32 v162, 0xffff, v161\n"
        "v_lshrrev_b32 v161, 16, v161\n"
        "v_lshlrev_b32 v162, v160, v162\n"
        "v_lshlrev_b32 v161, v160, v161\n"
        "v_cndmask_b32_e64 v158, v158, v162, s[62:63]\n"
        "v_cndmask_b32_e64 v159, v159, v161, s[62:63]\n"
        "s_nop 7\n"
        "s_branch .Lgf_a2_ret\n"
        ".Lgf_spl:\n"
        "s_nop 7\n"
        "global_load_dwordx4 v[160:163], v14, s[16:17] nt\n"
        "global_load_dwordx4 v[164:167], v14, s[18:19] nt\n"
        "global_load_dwordx4 v[168:171], v15, s[16:17] nt\n"
        "global_load_dwordx4 v[172:175], v15, s[18:19] nt\n"
        "global_load_ushort v22, v11, s[16:17]\n"
        "global_load_ushort v23, v11, s[18:19]\n"
        "global_load_ushort v20, v12, s[16:17]\n"
        "global_load_ushort v21, v12, s[18:19]\n"
        "v_add_u32 v14, 1050624, v14\n"
        "v_add_u32 v15, 1050624, v15\n"
        "v_add_u32 v11, 1050624, v11\n"
        "v_add_u32 v12, 1050624, v12\n"
        "global_load_dwordx4 v[120:123], v14, s[16:17] nt\n"
        "global_load_dwordx4 v[124:127], v14, s[18:19] nt\n"
        "global_load_dwordx4 v[128:131], v15, s[16:17] nt\n"
        "global_load_dwordx4 v[132:135], v15, s[18:19] nt\n"
        "global_load_ushort v42, v11, s[16:17]\n"
        "global_load_ushort v43, v11, s[18:19]\n"
        "global_load_ushort v40, v12, s[16:17]\n"
        "global_load_ushort v41, v12, s[18:19]\n"
        "s_waitcnt vmcnt(23)\n"
        "v_perm_b32 v176, v226, v224, s50\n"
        "v_perm_b32 v184, v227, v225, s50\n"
        "v_perm_b32 v180, v226, v224, s51\n"
        "v_perm_b32 v188, v227, v225, s51\n"
        "s_waitcnt vmcnt(21)\n"
        "v_perm_b32 v177, v230, v228, s50\n"
        "v_perm_b32 v185, v231, v229, s50\n"
        "v_perm_b32 v181, v230, v228, s51\n"
        "v_perm_b32 v189, v231, v229, s51\n"
        "s_waitcnt vmcnt(19)\n"
        "v_perm_b32 v178, v234, v232, s50\n"
        "v_perm_b32 v186, v235, v233, s50\n"
        "v_perm_b32 v182, v234, v232, s51\n"
        "v_perm_b32 v190, v235, v233, s51\n"
        "s_waitcnt vmcnt(17)\n"
        "v_perm_b32 v179, v238, v236, s50\n"
        "v_perm_b32 v187, v239, v237, s50\n"
        "v_perm_b32 v183, v238, v236, s51\n"
        "v_perm_b32 v191, v239, v237, s51\n"
        "s_nop 7\n"
        "s_branch .Lgf_spl_ret\n"
        ".Lgf_spb:\n"
        "s_nop 7\n"
        "v_mov_b32_dpp v40, v128 wave_shl:1 row_mask:0xf bank_mask:0xf\n"
        "v_mov_b32 v19, v128\n"
        "v_alignbit_b32 v128, v129, v128, 16\n"
        "v_alignbit_b32 v129, v130, v129, 16\n"
        "v_alignbit_b32 v130, v131, v130, 16\n"
        "v_alignbit_b32 v131, v40, v131, 16\n"
        "v_mov_b32 v40, v19\n"
        "v_mov_b32_dpp v41, v132 wave_shl:1 row_mask:0xf bank_mask:0xf\n"
        "v_mov_b32 v19, v132\n"
        "v_alignbit_b32 v132, v133, v132, 16\n"
        "v_alignbit_b32 v133, v134, v133, 16\n"
        "v_alignbit_b32 v134, v135, v134, 16\n"
        "v_alignbit_b32 v135, v41, v135, 16\n"
        "v_mov_b32 v41, v19\n"
        "ds_write_b128 v16, v[120:123] offset:0\n"
        "ds_write_b128 v16, v[124:127] offset:1024\n"
        "ds_write_b128 v16, v[128:131] offset:2048\n"
        "ds_write_b128 v16, v[132:135] offset:3072\n"
        "ds_read_b128 v[120:123], v17 offset:0\n"
        "ds_read_b128 v[124:127], v17 offset:1024\n"
        "ds_read_b128 v[128:131], v18 offset:2048\n"
        "ds_read_b128 v[132:135], v18 offset:3072\n"
        "s_waitcnt lgkmcnt(0)\n"
        "v_mfma_f32_16x16x32_bf16 v[192:195], v[120:123], v[144:147], 0\n"
        "v_mfma_f32_16x16x32_bf16 v[200:203], v[120:123], v[148:151], 0\n"
        "v_mfma_f32_16x16x32_bf16 v[196:199], v[124:127], v[144:147], 0\n"
        "v_mfma_f32_16x16x32_bf16 v[204:207], v[124:127], v[148:151], 0\n"
        "v_mfma_f32_16x16x32_bf16 v[208:211], v[128:131], v[152:155], 0\n"
        "v_mfma_f32_16x16x32_bf16 v[216:219], v[128:131], v[156:159], 0\n"
        "v_mfma_f32_16x16x32_bf16 v[212:215], v[132:135], v[152:155], 0\n"
        "v_mfma_f32_16x16x32_bf16 v[220:223], v[132:135], v[156:159], 0\n"
        "s_nop 7\n"
        "s_branch .Lgf_spb_ret\n"
        ".Lgf_spq:\n"
        "s_nop 7\n"
        "v_bfe_u32 v176, v1, 4, 4\n"
        "v_lshrrev_b32 v177, 8, v1\n"
        "v_mul_u32_u24 v14, 144, v176\n"
        "v_cmp_eq_u32 vcc, 0, v176\n"
        "v_lshl_add_u32 v14, v177, 4, v14\n"
        "v_sub_u32 v16, 4512, v14\n"
        "v_sub_u32 v15, 2352, v14\n"
        "v_sub_u32 v176, 52, v14\n"
        "v_cndmask_b32 v15, v15, v176, vcc\n"
        "v_lshlrev_b32 v40, 16, v40\n"
        "v_lshlrev_b32 v41, 16, v41\n"
        "v_lshlrev_b32 v42, 16, v42\n"
        "v_lshlrev_b32 v43, 16, v43\n"
        "s_mov_b64 exec, 1\n"
        "v_add_f32 v192, v192, v40\n"
        "v_sub_f32 v196, v196, v41\n"
        "v_add_f32 v208, v208, v42\n"
        "v_sub_f32 v212, v212, v43\n"
        "v_mul_f32 v192, 0.5, v192\n"
        "v_mul_f32 v196, 0.5, v196\n"
        "v_mul_f32 v208, 0.5, v208\n"
        "v_mul_f32 v212, 0.5, v212\n"
        "s_mov_b64 exec, -1\n"
        "v_pk_add_f32 v[144:145], v[112:113], v[192:193] neg_lo:[0,1] neg_hi:[0,1]\n"
        "v_pk_add_f32 v[112:113], v[112:113], v[192:193]\n"
        "v_pk_add_f32 v[146:147], v[114:115], v[194:195] neg_lo:[0,1] neg_hi:[0,1]\n"
        "v_pk_add_f32 v[114:115], v[114:115], v[194:195]\n"
        "v_pk_add_f32 v[148:149], v[116:117], v[196:197] neg_lo:[0,1] neg_hi:[0,1]\n"
        "v_pk_add_f32 v[116:117], v[116:117], v[196:197]\n"
        "v_pk_add_f32 v[150:151], v[118:119], v[198:199] neg_lo:[0,1] neg_hi:[0,1]\n"
        "v_pk_add_f32 v[118:119], v[118:119], v[198:199]\n"
        "v_pk_add_f32 v[152:153], v[120:121], v[200:201] neg_lo:[0,1] neg_hi:[0,1]\n"
        "v_pk_add_f32 v[120:121], v[120:121], v[200:201]\n"
        "v_pk_add_f32 v[154:155], v[122:123], v[202:203] neg_lo:[0,1] neg_hi:[0,1]\n"
        "v_pk_add_f32 v[122:123], v[122:123], v[202:203]\n"
        "v_pk_add_f32 v[156:157], v[124:125], v[204:205] neg_lo:[0,1] neg_hi:[0,1]\n"
        "v_pk_add_f32 v[124:125], v[124:125], v[204:205]\n"
        "v_pk_add_f32 v[158:159], v[126:127], v[206:207] neg_lo:[0,1] neg_hi:[0,1]\n"
        "v_pk_add_f32 v[126:127], v[126:127], v[206:207]\n"
        "v_pk_add_f32 v[160:161], v[128:129], v[208:209] neg_lo:[0,1] neg_hi:[0,1]\n"
        "v_pk_add_f32 v[128:129], v[128:129], v[208:209]\n"
        "v_pk_add_f32 v[162:163], v[130:131], v[210:211] neg_lo:[0,1] neg_hi:[0,1]\n"
        "v_pk_add_f32 v[130:131], v[130:131], v[210:211]\n"
        "v_pk_add_f32 v[164:165], v[132:133], v[212:213] neg_lo:[0,1] neg_hi:[0,1]\n"
        "v_pk_add_f32 v[132:133], v[132:133], v[212:213]\n"
        "v_pk_add_f32 v[166:167], v[134:135], v[214:215] neg_lo:[0,1] neg_hi:[0,1]\n"
        "v_pk_add_f32 v[134:135], v[134:135], v[214:215]\n"
        "v_pk_add_f32 v[168:169], v[136:137], v[216:217] neg_lo:[0,1] neg_hi:[0,1]\n"
        "v_pk_add_f32 v[136:137], v[136:137], v[216:217]\n"
        "v_pk_add_f32 v[170:171], v[138:139], v[218:219] neg_lo:[0,1] neg_hi:[0,1]\n"
        "v_pk_add_f32 v[138:139], v[138:139], v[218:219]\n"
        "v_pk_add_f32 v[172:173], v[140:141], v[220:221] neg_lo:[0,1] neg_hi:[0,1]\n"
        "v_pk_add_f32 v[140:141], v[140:141], v[220:221]\n"
        "v_pk_add_f32 v[174:175], v[142:143], v[222:223] neg_lo:[0,1] neg_hi:[0,1]\n"
        "v_pk_add_f32 v[142:143], v[142:143], v[222:223]\n"
        "s_nop 7\n"
        "s_branch .Lgf_spq_ret\n"
        ".Lgf_spz:\n"
        "s_nop 7\n"
        "ds_write_b128 v14, v[224:227] offset:24576\n"
        "ds_write_b128 v14, v[228:231] offset:29184\n"
        "ds_write_b128 v14, v[232:235] offset:24640\n"
        "ds_write_b128 v14, v[236:239] offset:29248\n"
        "ds_write_b128 v14, v[240:243] offset:26880\n"
        "ds_write_b128 v14, v[244:247] offset:31488\n"
        "ds_write_b128 v14, v[248:251] offset:26944\n"
        "ds_write_b128 v14, v[252:255] offset:31552\n"
        "s_mov_b64 exec, 1\n"
        "ds_write_b32 v14, v224 offset:24704\n"
        "ds_write_b32 v14, v228 offset:29312\n"
        "s_mov_b64 exec, -1\n"
        "ds_read_b128 v[176:179], v15 offset:24640\n"
        "ds_read_b128 v[180:183], v15 offset:29248\n"
        "ds_read_b128 v[184:187], v15 offset:24576\n"
        "ds_read_b128 v[188:191], v15 offset:29184\n"
        "s_nop 7\n"
        "s_branch .Lgf_spz_ret\n"
        ".Lgf_spm:\n"
        "s_nop 7\n"
        "ds_read_b128 v[224:227], v16 offset:24640\n"
        "ds_read_b128 v[228:231], v16 offset:29248\n"
        "ds_read_b128 v[232:235], v16 offset:24576\n"
        "ds_read_b128 v[236:239], v16 offset:29184\n"
        "s_waitcnt lgkmcnt(4)\n"
        "v_pk_mul_f32 v[240:241], v[178:179], v[144:145] op_sel:[1,0] op_sel_hi:[0,1]\n"
        "v_pk_mul_f32 v[242:243], v[178:179], v[148:149] op_sel:[1,0] op_sel_hi:[0,1]\n"
        "v_pk_fma_f32 v[192:193], v[186:187], v[160:161], v[192:193] op_sel:[1,0,0] op_sel_hi:[0,1,1]\n"
        "v_pk_fma_f32 v[196:197], v[186:187], v[164:165], v[196:197] op_sel:[1,0,0] op_sel_hi:[0,1,1] neg_lo:[1,0,0] neg_hi:[1,0,0]\n"
        "v_pk_fma_f32 v[240:241], v[182:183], v[148:149], v[240:241] op_sel:[1,0,0] op_sel_hi:[0,1,1]\n"
        "v_pk_fma_f32 v[242:243], v[182:183], v[144:145], v[242:243] op_sel:[1,0,0] op_sel_hi:[0,1,1] neg_lo:[1,0,0] neg_hi:[1,0,0]\n"
        "v_pk_fma_f32 v[192:193], v[190:191], v[164:165], v[192:193] op_sel:[1,0,0] op_sel_hi:[0,1,1] neg_lo:[1,0,0] neg_hi:[1,0,0]\n"
        "v_pk_fma_f32 v[196:197], v[190:191], v[160:161], v[196:197] op_sel:[1,0,0] op_sel_hi:[0,1,1] neg_lo:[1,0,0] neg_hi:[1,0,0]\n"
        "v_pk_add_f32 v[200:201], v[240:241], v[200:201]\n"
        "v_pk_add_f32 v[204:205], v[242:243], v[204:205]\n"
        "v_pk_add_f32 v[192:193], v[240:241], v[192:193]\n"
        "v_pk_add_f32 v[196:197], v[242:243], v[196:197]\n"
        "v_pk_mul_f32 v[240:241], v[176:177], v[146:147] op_sel:[1,0] op_sel_hi:[0,1]\n"
        "v_pk_mul_f32 v[242:243], v[176:177], v[150:151] op_sel:[1,0] op_sel_hi:[0,1]\n"
        "v_pk_fma_f32 v[194:195], v[184:185], v[162:163], v[194:195] op_sel:[1,0,0] op_sel_hi:[0,1,1]\n"
        "v_pk_fma_f32 v[198:199], v[184:185], v[166:167], v[198:199] op_sel:[1,0,0] op_sel_hi:[0,1,1] neg_lo:[1,0,0] neg_hi:[1,0,0]\n"
        "v_pk_fma_f32 v[240:241], v[180:181], v[150:151], v[240:241] op_sel:[1,0,0] op_sel_hi:[0,1,1]\n"
        "v_pk_fma_f32 v[242:243], v[180:181], v[146:147], v[242:243] op_sel:[1,0,0] op_sel_hi:[0,1,1] neg_lo:[1,0,0] neg_hi:[1,0,0]\n"
        "v_pk_fma_f32 v[194:195], v[188:189], v[166:167], v[194:195] op_sel:[1,0,0] op_sel_hi:[0,1,1] neg_lo:[1,0,0] neg_hi:[1,0,0]\n"
        "v_pk_fma_f32 v[198:199], v[188:189], v[162:163], v[198:199] op_sel:[1,0,0] op_sel_hi:[0,1,1] neg_lo:[1,0,0] neg_hi:[1,0,0]\n"
        "v_pk_add_f32 v[202:203], v[240:241], v[202:203]\n"
        "v_pk_add_f32 v[206:207], v[242:243], v[206:207]\n"
        "v_pk_add_f32 v[194:195], v[240:241], v[194:195]\n"
        "v_pk_add_f32 v[198:199], v[242:243], v[198:199]\n"
        "s_waitcnt lgkmcnt(0)\n"
        "v_pk_mul_f32 v[240:241], v[226:227], v[152:153] op_sel:[1,0] op_sel_hi:[0,1]\n"
        "v_pk_mul_f32 v[242:243], v[226:227], v[156:157] op_sel:[1,0] op_sel_hi:[0,1]\n"
        "v_pk_fma_f32 v[208:209], v[234:235], v[168:169], v[208:209] op_sel:[1,0,0] op_sel_hi:[0,1,1]\n"
        "v_pk_fma_f32 v[212:213], v[234:235], v[172:173], v[212:213] op_sel:[1,0,0] op_sel_hi:[0,1,1] neg_lo:[1,0,0] neg_hi:[1,0,0]\n"
        "v_pk_fma_f32 v[240:241], v[230:231], v[156:157], v[240:241] op_sel:[1,0,0] op_sel_hi:[0,1,1]\n"
        "v_pk_fma_f32 v[242:243], v[230:231], v[152:153], v[242:243] op_sel:[1,0,0] op_sel_hi:[0,1,1] neg_lo:[1,0,0] neg_hi:[1,0,0]\n"
        "v_pk_fma_f32 v[208:209], v[238:239], v[172:173], v[208:209] op_sel:[1,0,0] op_sel_hi:[0,1,1] neg_lo:[1,0,0] neg_hi:[1,0,0]\n"
        "v_pk_fma_f32 v[212:213], v[238:239], v[168:169], v[212:213] op_sel:[1,0,0] op_sel_hi:[0,1,1] neg_lo:[1,0,0] neg_hi:[1,0,0]\n"
        "v_pk_add_f32 v[216:217], v[240:241], v[216:217]\n"
        "v_pk_add_f32 v[220:221], v[242:243], v[220:221]\n"
        "v_pk_add_f32 v[208:209], v[240:241], v[208:209]\n"
        "v_pk_add_f32 v[212:213], v[242:243], v[212:213]\n"
        "v_pk_mul_f32 v[240:241], v[224:225], v[154:155] op_sel:[1,0] op_sel_hi:[0,1]\n"
        "v_pk_mul_f32 v[242:243], v[224:225], v[158:159] op_sel:[1,0] op_sel_hi:[0,1]\n"
        "v_pk_fma_f32 v[210:211], v[232:233], v[170:171], v[210:211] op_sel:[1,0,0] op_sel_hi:[0,1,1]\n"
        "v_pk_fma_f32 v[214:215], v[232:233], v[174:175], v[214:215] op_sel:[1,0,0] op_sel_hi:[0,1,1] neg_lo:[1,0,0] neg_hi:[1,0,0]\n"
        "v_pk_fma_f32 v[240:241], v[228:229], v[158:159], v[240:241] op_sel:[1,0,0] op_sel_hi:[0,1,1]\n"
        "v_pk_fma_f32 v[242:243], v[228:229], v[154:155], v[242:243] op_sel:[1,0,0] op_sel_hi:[0,1,1] neg_lo:[1,0,0] neg_hi:[1,0,0]\n"
        "v_pk_fma_f32 v[210:211], v[236:237], v[174:175], v[210:211] op_sel:[1,0,0] op_sel_hi:[0,1,1] neg_lo:[1,0,0] neg_hi:[1,0,0]\n"
        "v_pk_fma_f32 v[214:215], v[236:237], v[170:171], v[214:215] op_sel:[1,0,0] op_sel_hi:[0,1,1] neg_lo:[1,0,0] neg_hi:[1,0,0]\n"
        "v_pk_add_f32 v[218:219], v[240:241], v[218:219]\n"
        "v_pk_add_f32 v[222:223], v[242:243], v[222:223]\n"
        "v_pk_add_f32 v[210:211], v[240:241], v[210:211]\n"
        "v_pk_add_f32 v[214:215], v[242:243], v[214:215]\n"
        "s_nop 7\n"
        "s_branch .Lgf_spm_ret\n"
        ".Lgf_ep:\n"
        "s_nop 7\n"
        "s_mov_b64 exec, 1\n"
        "buffer_store_dwordx2 v[6:7], off, s[12:15], s56 sc1\n"
        "s_mov_b64 exec, -1\n"
        "s_nop 7\n"
        "s_branch .Lgf_ep_ret\n"
        ".Lgf_end:\n"
        :: "{s[0:1]}"(__builtin_amdgcn_kernarg_segment_ptr()), "{s2}"(__builtin_amdgcn_workgroup_id_x()), "{v0}"(__builtin_amdgcn_workitem_id_x()),
           "s"((unsigned)(uintptr_t)(__attribute__((address_space(3))) char*)smem)
        : "memory", "vcc", "scc", "v1", "v2", "v3", "v4", "v5", "v6", "v7", "v8", "v9", "v10", "v11", "v12", "v13", "v14", "v15", "v16", "v17", "v18",
          "v19", "v20", "v21", "v22", "v23", "v24", "v25", "v26", "v27", "v28", "v29", "v30", "v31", "v32", "v33", "v34", "v35",
          "v36", "v37", "v38", "v39", "v40", "v41", "v42", "v43", "v44", "v45", "v46", "v47", "v48", "v49", "v50", "v51", "v52",
          "v53", "v54", "v55", "v56", "v57", "v58", "v59", "v60", "v61", "v62", "v63", "v64", "v65", "v66", "v67", "v68", "v69",
          "v70", "v71", "v72", "v73", "v74", "v75", "v76", "v77", "v78", "v79", "v80", "v81", "v82", "v83", "v84", "v85", "v86",
          "v87", "v88", "v89", "v90", "v91", "v92", "v93", "v94", "v95", "v96", "v97", "v98", "v99", "v100", "v101", "v102",
          "v103", "v104", "v105", "v106", "v107", "v108", "v109", "v110", "v111", "v112", "v113", "v114", "v115", "v116", "v117",
          "v118", "v119", "v120", "v121", "v122", "v123", "v124", "v125", "v126", "v127", "v128", "v129", "v130", "v131", "v132",
          "v133", "v134", "v135", "v136", "v137", "v138", "v139", "v140", "v141", "v142", "v143", "v144", "v145", "v146", "v147",
          "v148", "v149", "v150", "v151", "v152", "v153", "v154", "v155", "v156", "v157", "v158", "v159", "v160", "v161", "v162",
          "v163", "v164", "v165", "v166", "v167", "v168", "v169", "v170", "v171", "v172", "v173", "v174", "v175", "v176", "v177",
          "v178", "v179", "v180", "v181", "v182", "v183", "v184", "v185", "v186", "v187", "v188", "v189", "v190", "v191", "v192",
          "v193", "v194", "v195", "v196", "v197", "v198", "v199", "v200", "v201", "v202", "v203", "v204", "v205", "v206", "v207",
          "v208", "v209", "v210", "v211", "v212", "v213", "v214", "v215", "v216", "v217", "v218", "v219", "v220", "v221", "v222",
          "v223", "v224", "v225", "v226", "v227", "v228", "v229", "v230", "v231", "v232", "v233", "v234", "v235", "v236", "v237",
          "v238", "v239", "v240", "v241", "v242", "v243", "v244", "v245", "v246", "v247", "v248", "v249", "v250", "v251", "v252",
          "v253", "v254", "v255", "s4", "s5", "s6", "s7", "s8", "s9", "s10", "s11", "s12", "s13", "s14", "s15", "s16", "s17",
          "s18", "s19", "s20", "s21", "s22", "s23", "s24", "s25", "s26", "s27", "s28", "s29", "s30", "s31", "s34", "s35", "s36",
          "s37", "s38", "s39", "s40", "s41", "s42", "s43", "s44", "s45", "s46", "s47", "s48", "s49", "s50", "s51", "s52", "s53",
          "s54", "s55", "s56", "s57", "s58", "s59", "s60", "s61", "s62", "s63", "s64", "s65", "s66", "s67", "s68", "s69", "s70",
          "s71", "s72", "s73", "s74");
}

extern "C" void kernel_launch(void* const* d_in, const int* in_sizes, int n_in, void* d_out, int out_size, void* d_ws, size_t ws_size,
                              hipStream_t stream) {
    Args A;
    for (int h = 0; h < 2; ++h) {
        A.h[h].x = Srd{d_in[3 * h], 2048u * 1024u * 2u, 0x00020000u};
        A.h[h].out = Srd{(char*)d_out + 2048 * h, 2048u * 2048u * 2u - 2048u * h, 0x00020000u};
        A.h[h].ws = Srd{(char*)d_ws + WS_OFF + (size_t)WS_HEAD * h, WS_HEAD, 0x00020000u};
        A.h[h].wr = d_in[3 * h + 1]; A.h[h].wi = d_in[3 * h + 2];
    }
    gfnet2_kernel<<<256, 512, 0, stream>>>(A);
}
```
